```python
import torch

def _softmax(x, axis=-1):
    return torch.softmax(x, dim=axis)
def _gelu(x):
    return torch.nn.functional.gelu(x, approximate="tanh")
def _silu(x):
    return torch.nn.functional.silu(x)
def _sigmoid(x):
    return torch.sigmoid(x)
def _layernorm(x, axis=-1, eps=1e-5):
    m = x.mean(dim=axis, keepdim=True)
    v = x.var(dim=axis, keepdim=True, unbiased=False)
    return (x - m) / torch.sqrt(v + eps)
def _scatter_add(tgt, idx, src, axis=0):
    return tgt.clone().index_add_(axis, idx, src)
def _segment_sum(src, seg, n):
    out = torch.zeros((n,) + src.shape[1:], dtype=src.dtype)
    return out.index_add_(0, seg, src)
def _pad(x, pw):
    flat = tuple(v for pair in reversed(pw) for v in pair)
    return torch.nn.functional.pad(x, flat)

kernel_name = 'standalone__kv_cache_quant_int4_int2_kv_with_per_channel_or_per_token_scales__decode'

def setup_inputs():

    _w = (lambda a: (lambda t: t.to(torch.bfloat16) if t.is_floating_point() else t)(torch.as_tensor(a)))
    B = 1
    Sq = 1
    Skv = 65536
    Hq = 32
    Hkv = 4
    Dh = 128
    bits = 2
    G = 128
    if G <= 0 or G >= Skv:
        G = max(1, Skv // 2)
    Skv = (Skv // G) * G
    ngk = Skv // G
    qmax = (1 << bits) - 1
    return {
        "q": _w((lambda *s: torch.randn(*s, dtype=torch.bfloat16) * 3.2)(B, Hq, Sq, Dh)),
        "qk": _w(torch.randint(0, qmax + 1, (B, Hkv, Skv, Dh)).to(torch.int32)),
        "qv": _w(torch.randint(0, qmax + 1, (B, Hkv, Skv, Dh)).to(torch.int32)),
        "k_scales": _w(torch.abs((lambda *s: torch.randn(*s, dtype=torch.bfloat16) * 3.2)(B, Hkv, ngk, Dh)) * 0.05 + 0.02),
        "k_zeros": _w((lambda *s: torch.randn(*s, dtype=torch.bfloat16) * 3.2)(B, Hkv, ngk, Dh) + float(qmax) / 2.0),
        "v_scales": _w(torch.abs((lambda *s: torch.randn(*s, dtype=torch.bfloat16) * 0.05)(B, Hkv, Skv, 1)) * 0.05 + 0.02),
        "v_zeros": _w((lambda *s: torch.randn(*s, dtype=torch.bfloat16) * 0.05)(B, Hkv, Skv, 1) + float(qmax) / 2.0),
    }

def reference(q, qk, qv, k_scales, k_zeros, v_scales, v_zeros):
    B, Hq, Sq, Dh = q.shape
    Hkv, Skv = qk.shape[1], qk.shape[2]
    ngk = k_scales.shape[2]
    G = Skv // ngk
    ks = (lambda x, n, axis: torch.repeat_interleave(x, n, dim=axis))(k_scales, G, axis=2)
    kz = (lambda x, n, axis: torch.repeat_interleave(x, n, dim=axis))(k_zeros, G, axis=2)
    k = ((lambda x, dt: x.to(dt))(qk, q.dtype) - kz) * ks
    v = ((lambda x, dt: x.to(dt))(qv, q.dtype) - v_zeros) * v_scales
    rep = Hq // Hkv
    if rep > 1:
        k = (lambda x, n, axis: torch.repeat_interleave(x, n, dim=axis))(k, rep, axis=1)
        v = (lambda x, n, axis: torch.repeat_interleave(x, n, dim=axis))(v, rep, axis=1)
    scale = float(Dh) ** -0.5
    logits = torch.einsum("bhqd,bhkd->bhqk", q, k) * scale
    p = _softmax(logits, axis=-1)
    o = torch.einsum("bhqk,bhkd->bhqd", p, v)
    return o
```

```cpp
#include <hip/hip_runtime.h>
#include <hip/hip_bf16.h>
#include <stdint.h>

#define NWAVE 4
#define WIN 16
#define POLL_SLEEP 4
#define SMALL_MODE 1
#define LDS_PAD 0

namespace {
constexpr int HQ = 32, HKV = 4, SKV = 65536, DH = 128, GS = 128, NGRP = SKV / GS;
constexpr int NTHR = NWAVE * 64;
constexpr int NSPLIT = 64;
constexpr int GPI = NWAVE / 4;
constexpr int NIT = NGRP / (NSPLIT * GPI);
constexpr int NPART = 2 * NWAVE;
constexpr int OSTR = 132;
constexpr int WBYTES = 16 * OSTR * 4;
constexpr size_t WS_INBOX = 4096;
constexpr size_t WS_PAY = WS_INBOX + (size_t)HKV * NSPLIT * NSPLIT * 8;

typedef unsigned v4u __attribute__((ext_vector_type(4)));
typedef unsigned v2u __attribute__((ext_vector_type(2)));
typedef float f32x4 __attribute__((ext_vector_type(4)));
typedef float f32x2 __attribute__((ext_vector_type(2)));
typedef __bf16 bf16x8 __attribute__((ext_vector_type(8)));
typedef __bf16 bf16x2 __attribute__((ext_vector_type(2)));
#define LAS __attribute__((address_space(3)))
#define GAS __attribute__((address_space(1)))
typedef GAS unsigned gu32;
typedef GAS unsigned long long gu64;
#define RLX_AGENT __ATOMIC_RELAXED, __HIP_MEMORY_SCOPE_AGENT

__device__ __forceinline__ unsigned pk_bf16(float lo, float hi) {
  f32x2 v = {lo, hi};
  return __builtin_bit_cast(unsigned, __builtin_convertvector(v, bf16x2));
}
__device__ __forceinline__ float bf_lo(unsigned p) { return __builtin_bit_cast(float, p << 16); }
__device__ __forceinline__ float bf_hi(unsigned p) { return __builtin_bit_cast(float, p & 0xffff0000u); }
__device__ __forceinline__ f32x4 mfma16(v4u a, v4u b, f32x4 c) {
  return __builtin_amdgcn_mfma_f32_16x16x32_bf16(__builtin_bit_cast(bf16x8, a), __builtin_bit_cast(bf16x8, b), c, 0, 0, 0);
}
__device__ __forceinline__ v4u ldnt(__amdgpu_buffer_rsrc_t rs, int voff, int soff) {
  return __builtin_amdgcn_raw_buffer_load_b128(rs, voff, soff, 2);
}
template <int CTRL> __device__ __forceinline__ float dpp(float x) {
  return __builtin_bit_cast(float, __builtin_amdgcn_mov_dpp(__builtin_bit_cast(int, x), CTRL, 0xf, 0xf, true));
}
__device__ __forceinline__ float xrow_max(float x) {
  auto s = __builtin_amdgcn_permlane16_swap(__float_as_uint(x), __float_as_uint(x), false, false);
  x = fmaxf(__uint_as_float(s[0]), __uint_as_float(s[1]));
  auto t = __builtin_amdgcn_permlane32_swap(__float_as_uint(x), __float_as_uint(x), false, false);
  return fmaxf(__uint_as_float(t[0]), __uint_as_float(t[1]));
}
__device__ __forceinline__ float xrow_sum(float x) {
  auto s = __builtin_amdgcn_permlane16_swap(__float_as_uint(x), __float_as_uint(x), false, false);
  x = __uint_as_float(s[0]) + __uint_as_float(s[1]);
  auto t = __builtin_amdgcn_permlane32_swap(__float_as_uint(x), __float_as_uint(x), false, false);
  return __uint_as_float(t[0]) + __uint_as_float(t[1]);
}
__device__ __forceinline__ v2u ldsmall(const unsigned short* p) {
  return __builtin_nontemporal_load((const v2u*)p);
}
__device__ __forceinline__ void put_granule(gu64* g, unsigned tag, float v) {
  __hip_atomic_store(g, ((unsigned long long)tag << 32) | __builtin_bit_cast(unsigned, v), RLX_AGENT);
}

constexpr float LOG2E = 1.4426950408889634f;
constexpr float SCALE = 0.08838834764831845f;

__global__ void __launch_bounds__(NTHR, 2)
kv_decode(const unsigned short* __restrict__ q, const int* __restrict__ qk, const int* __restrict__ qv,
          const unsigned short* __restrict__ ks, const unsigned short* __restrict__ kz,
          const unsigned short* __restrict__ vs, const unsigned short* __restrict__ vz,
          unsigned char* ws, unsigned short* __restrict__ out) {
  __shared__ __attribute__((aligned(16))) unsigned char smem[NWAVE * WBYTES + NPART * 8 * 16 + NWAVE * 32 * 4 + LDS_PAD];
  const int tid = threadIdx.x, lane = tid & 63;
  const int w = __builtin_amdgcn_readfirstlane(tid >> 6);
  const int hk = blockIdx.x / NSPLIT, cb = blockIdx.x % NSPLIT;
  const int g = lane >> 4, c = lane & 15, u = (lane >> 3) & 1, h = lane & 7;
  const int wq = w & 3;
  const int rot = ((blockIdx.x >> 3) + 2 * w) & 7;
  constexpr int HEADB = SKV * DH * 4;
  const char* kh = (const char*)qk + (size_t)hk * HEADB;
  const char* vh = (const char*)qv + (size_t)hk * HEADB;
  const __amdgpu_buffer_rsrc_t rk = __builtin_amdgcn_make_buffer_rsrc((void*)kh, 0, HEADB, 0x00020000);
  const __amdgpu_buffer_rsrc_t rv = __builtin_amdgcn_make_buffer_rsrc((void*)vh, 0, HEADB, 0x00020000);
  const int wbase = (wq >> 1) * 32768 + (wq & 1) * 2048;
  const int voffK = lane * 16;
  const int voffV0 = ((g ^ rot) << 12) + c * 16, voffV1 = (((4 + g) ^ rot) << 12) + c * 16;
  int grp = cb * GPI + (w >> 2);
  const unsigned short* ksp = ks + (size_t)hk * NGRP * DH + 4 * (lane & 31);
  const unsigned short* kzp = kz + (size_t)hk * NGRP * DH + 4 * (lane & 31);
  const int tokl = 64 * (wq >> 1) + 8 * ((4 * u + g) ^ rot) + 4 * (wq & 1);
  const unsigned short* vsp = vs + (size_t)hk * SKV + tokl;
  const unsigned short* vzp = vz + (size_t)hk * SKV + tokl;

#define LDK(rs, gb, p) ldnt(rs, voffK + ((p) & 1) * 1024, (gb) + ((((p) >> 1) ^ rot) << 12))
#define LDV(rs, gb, n) ldnt(rs, (((n) & 4) ? voffV1 : voffV0) + ((n) & 3) * 512 + ((n) >> 3) * 256, (gb))

  v2u kzr = ldsmall(kzp + grp * DH);
  v2u ksr = ldsmall(ksp + grp * DH);
  v4u a[WIN];
  {
    const int gb = grp * 65536 + wbase;
#pragma unroll
    for (int i = 0; i < 16; ++i) a[i] = LDK(rk, gb, i);
    __builtin_amdgcn_sched_barrier(0);
  }
  v4u qf[4];
#pragma unroll
  for (int kk = 0; kk < 4; ++kk) qf[kk] = __builtin_nontemporal_load((const v4u*)(q + (size_t)(hk * 8 + h) * DH + 32 * kk + 8 * g));
  v2u vsr = ldsmall(vsp + grp * GS);
  v2u vzr = ldsmall(vzp + grp * GS);
  gu32* epoch = (gu32*)ws + hk * 64;
  const unsigned tag = __hip_atomic_load(epoch, RLX_AGENT) + 1u;
  __builtin_amdgcn_sched_barrier(0);
  if (WIN == 32) {
    const int gb = grp * 65536 + wbase;
#pragma unroll
    for (int i = 0; i < 16; ++i) a[(16 + i) % WIN] = LDV(rv, gb, i);
    __builtin_amdgcn_sched_barrier(0);
  }

  LAS unsigned char* kbuf = (LAS unsigned char*)smem + w * WBYTES;
  const int hi = lane >> 5;
  const int wr0 = hi * 256 + ((((lane & 31) >> 1) ^ hi) << 4) + 8 * (lane & 1);
  float m = -INFINITY, lsum = 0.f, csum = 0.f;
  f32x4 acc[2][4];
#pragma unroll
  for (int hf = 0; hf < 2; ++hf)
#pragma unroll
    for (int e = 0; e < 4; ++e) acc[hf][e] = f32x4{0.f, 0.f, 0.f, 0.f};

  for (int it = 0; it < NIT; ++it) {
    const int gb = grp * 65536 + wbase;
    const bool more = it + 1 < NIT;
    const int grpn = more ? grp + NSPLIT * GPI : grp;
#define grpS grpn
    const int gbn = grpn * 65536 + wbase;
    const __amdgpu_buffer_rsrc_t rkn = __builtin_amdgcn_make_buffer_rsrc((void*)kh, 0, more ? HEADB : 0, 0x00020000);
    const __amdgpu_buffer_rsrc_t rvn = __builtin_amdgcn_make_buffer_rsrc((void*)vh, 0, more ? HEADB : 0, 0x00020000);
    const float kz0 = bf_lo(kzr.x), kz1 = bf_hi(kzr.x), kz2 = bf_lo(kzr.y), kz3 = bf_hi(kzr.y);
    const float ks0 = bf_lo(ksr.x), ks1 = bf_hi(ksr.x), ks2 = bf_lo(ksr.y), ks3 = bf_hi(ksr.y);
    if (WIN == 32) {
      kzr = ldsmall(kzp + grpS * DH);
      ksr = ldsmall(ksp + grpS * DH);
    }
    __builtin_amdgcn_sched_barrier(0);
#pragma unroll
    for (int p = 0; p < 16; ++p) {
      const v4u x = a[p];
      const unsigned d01 = pk_bf16((float)(int)x.x - kz0, (float)(int)x.y - kz1);
      const unsigned d23 = pk_bf16((float)(int)x.z - kz2, (float)(int)x.w - kz3);
      asm volatile("" ::"v"(d01), "v"(d23));
      a[p] = WIN == 32 ? LDK(rkn, gbn, p) : LDV(rv, gb, p);
      v2u kk2;
      kk2.x = pk_bf16(bf_lo(d01) * ks0, bf_hi(d01) * ks1);
      kk2.y = pk_bf16(bf_lo(d23) * ks2, bf_hi(d23) * ks3);
      *(LAS v2u*)(kbuf + (wr0 ^ (32 * (p & 7))) + 512 * p) = kk2;
      __builtin_amdgcn_sched_barrier(0);
    }
    f32x4 d0 = {0.f, 0.f, 0.f, 0.f}, d1 = {0.f, 0.f, 0.f, 0.f};
#pragma unroll
    for (int kk = 0; kk < 4; ++kk) {
      const int ra = c * 256 + (((4 * kk + g) ^ c) << 4);
      const v4u k0 = *(LAS v4u*)(kbuf + ra);
      const v4u k1 = *(LAS v4u*)(kbuf + ra + 4096);
      d0 = mfma16(k0, qf[kk], d0);
      d1 = mfma16(k1, qf[kk], d1);
    }
    float s[4];
    {
      const unsigned r01 = pk_bf16(u ? d1[0] : d0[0], u ? d1[1] : d0[1]);
      const unsigned r23 = pk_bf16(u ? d1[2] : d0[2], u ? d1[3] : d0[3]);
      const unsigned s01 = pk_bf16(bf_lo(r01) * SCALE, bf_hi(r01) * SCALE);
      const unsigned s23 = pk_bf16(bf_lo(r23) * SCALE, bf_hi(r23) * SCALE);
      s[0] = bf_lo(s01); s[1] = bf_hi(s01); s[2] = bf_lo(s23); s[3] = bf_hi(s23);
    }
    const float mn = fmaxf(m, xrow_max(fmaxf(fmaxf(s[0], s[1]), fmaxf(s[2], s[3]))));
    const float alpha = __builtin_amdgcn_exp2f((m - mn) * LOG2E);
    m = mn;
    float p[4];
#pragma unroll
    for (int r = 0; r < 4; ++r) p[r] = __builtin_amdgcn_exp2f((s[r] - mn) * LOG2E);
    lsum = fmaf(lsum, alpha, (p[0] + p[1]) + (p[2] + p[3]));
    const unsigned p01 = pk_bf16(p[0] * bf_lo(vsr.x), p[1] * bf_hi(vsr.x));
    const unsigned p23 = pk_bf16(p[2] * bf_lo(vsr.y), p[3] * bf_hi(vsr.y));
    csum *= alpha;
    csum = fmaf(bf_lo(p01), bf_lo(vzr.x), csum);
    csum = fmaf(bf_hi(p01), bf_hi(vzr.x), csum);
    csum = fmaf(bf_lo(p23), bf_lo(vzr.y), csum);
    csum = fmaf(bf_hi(p23), bf_hi(vzr.y), csum);
    v4u pf;
    pf.x = u ? 0u : p01;
    pf.y = u ? 0u : p23;
    pf.z = u ? p01 : 0u;
    pf.w = u ? p23 : 0u;
#pragma unroll
    for (int hf = 0; hf < 2; ++hf)
#pragma unroll
      for (int e = 0; e < 4; ++e) acc[hf][e] *= alpha;
    __builtin_amdgcn_sched_barrier(0);
    if (WIN == 16) {
      kzr = ldsmall(kzp + grpS * DH);
      ksr = ldsmall(ksp + grpS * DH);
    }
    vsr = ldsmall(vsp + grpS * GS);
    vzr = ldsmall(vzp + grpS * GS);
    __builtin_amdgcn_sched_barrier(0);
#pragma unroll
    for (int hf = 0; hf < 2; ++hf) {
      v4u vf[4];
#pragma unroll
      for (int jp = 0; jp < 4; ++jp) {
        const int n = hf * 8 + 2 * jp, sl = (WIN - 16) + n;
        const v4u x0 = a[sl], x1 = a[sl + 1];
#pragma unroll
        for (int e = 0; e < 4; ++e) vf[e][jp] = pk_bf16((float)(int)x0[e], (float)(int)x1[e]);
        asm volatile("" ::"v"(vf[0][jp]), "v"(vf[1][jp]), "v"(vf[2][jp]), "v"(vf[3][jp]));
        a[sl] = WIN == 32 ? LDV(rvn, gbn, n) : LDK(rkn, gbn, n);
        a[sl + 1] = WIN == 32 ? LDV(rvn, gbn, n + 1) : LDK(rkn, gbn, n + 1);
        __builtin_amdgcn_sched_barrier(0);
      }
#pragma unroll
      for (int e = 0; e < 4; ++e) acc[hf][e] = mfma16(vf[e], pf, acc[hf][e]);
      __builtin_amdgcn_sched_barrier(0);
    }
    grp = grpn;
  }
  lsum = xrow_sum(lsum);
  csum = xrow_sum(csum);
  LAS float* obuf = (LAS float*)kbuf;
#pragma unroll
  for (int hf = 0; hf < 2; ++hf)
#pragma unroll
    for (int r = 0; r < 4; ++r) {
      const f32x4 o = {acc[hf][0][r], acc[hf][1][r], acc[hf][2][r], acc[hf][3][r]};
      *(LAS f32x4*)(obuf + c * OSTR + 64 * hf + 16 * g + 4 * r) = o;
    }
  LAS f32x4* mbuf = (LAS f32x4*)((LAS unsigned char*)smem + NWAVE * WBYTES);
  if (lane < 16) mbuf[(w * 2 + u) * 8 + h] = f32x4{m, lsum, csum, 0.f};
  __syncthreads();
  const __amdgpu_buffer_rsrc_t rp =
      __builtin_amdgcn_make_buffer_rsrc((void*)(ws + WS_PAY + (size_t)hk * NSPLIT * NSPLIT * 128), 0, NSPLIT * NSPLIT * 128, 0x00020000);
  gu64* inbox = (gu64*)(ws + WS_INBOX) + (size_t)hk * NSPLIT * NSPLIT;
#pragma unroll
  for (int k = 0; k < 8 / NWAVE; ++k) {
    const int hh = w + NWAVE * k, d0 = 2 * lane;
    float M = -INFINITY;
#pragma unroll
    for (int i = 0; i < NPART; ++i) M = fmaxf(M, mbuf[i * 8 + hh][0]);
    float L = 0.f, o0 = 0.f, o1 = 0.f;
#pragma unroll
    for (int i = 0; i < NPART; ++i) {
      const f32x4 ml = mbuf[i * 8 + hh];
      const float e = __builtin_amdgcn_exp2f((ml[0] - M) * LOG2E);
      const f32x2 ob = *(const LAS f32x2*)((const LAS float*)((LAS unsigned char*)smem + (i >> 1) * WBYTES) + ((i & 1) * 8 + hh) * OSTR + d0);
      L = fmaf(e, ml[1], L);
      o0 = fmaf(e, ob[0] - ml[2], o0);
      o1 = fmaf(e, ob[1] - ml[2], o1);
    }
    const float inv = 1.0f / L;
    const v4u pk = {__builtin_bit_cast(unsigned, o0 * inv), tag, __builtin_bit_cast(unsigned, o1 * inv), tag};
    __builtin_amdgcn_raw_buffer_store_b128(pk, rp, ((hh * 8 + (lane >> 3)) * NSPLIT + cb) * 128 + (lane & 7) * 16, 0, 16);
    if (lane < 8) put_granule(inbox + (hh * 8 + lane) * NSPLIT + cb, tag, fmaf(M, LOG2E, __builtin_amdgcn_logf(L)));
  }
  {
    constexpr int TPP = NTHR / NSPLIT;
    constexpr int NLD = 8 / TPP;
    const int pr = tid / TPP, part = tid % TPP;
    const int poff = (cb * NSPLIT + pr) * 128 + part * NLD * 16;
    gu64* ib = inbox + cb * NSPLIT + pr;
    float o[2 * NLD], lse = 0.f;
    bool have = false, seen = false, fetch = true;
    unsigned spins = 0;
    while (true) {
      unsigned long long f = 0;
      if (!seen) f = __hip_atomic_load(ib, RLX_AGENT);
      if (!have && fetch) {
        v4u x[NLD];
#pragma unroll
        for (int j = 0; j < NLD; ++j) x[j] = __builtin_amdgcn_raw_buffer_load_b128(rp, poff + 16 * j, 0, 16);
        have = true;
#pragma unroll
        for (int j = 0; j < NLD; ++j) {
          have &= (x[j].y == tag) & (x[j].w == tag);
          o[2 * j] = __uint_as_float(x[j].x);
          o[2 * j + 1] = __uint_as_float(x[j].z);
        }
      }
      if (!seen) {
        seen = (unsigned)(f >> 32) == tag;
        lse = __builtin_bit_cast(float, (unsigned)f);
      }
      if (__all(have && seen)) break;
      if (++spins > (1u << 20)) { lse = __builtin_nanf(""); break; }
      const int miss = __builtin_popcountll(__ballot(!seen));
      fetch = seen || miss <= 2 * TPP;
      __builtin_amdgcn_s_sleep(POLL_SLEEP);
    }
    float Mw = lse;
#pragma unroll
    for (int off = TPP; off < 64; off <<= 1) Mw = fmaxf(Mw, __shfl_xor(Mw, off));
    float L = __builtin_amdgcn_exp2f(lse - Mw);
#pragma unroll
    for (int j = 0; j < 2 * NLD; ++j) o[j] *= L;
#pragma unroll
    for (int off = TPP; off < 64; off <<= 1) {
      L += __shfl_xor(L, off);
#pragma unroll
      for (int j = 0; j < 2 * NLD; ++j) o[j] += __shfl_xor(o[j], off);
    }
    LAS float* fin = (LAS float*)((LAS unsigned char*)smem + NWAVE * WBYTES + NPART * 8 * 16);
    if (lane < TPP) {
#pragma unroll
      for (int j = 0; j < 2 * NLD; ++j) fin[w * 32 + lane * 2 * NLD + j] = o[j];
      if (lane == 0) { fin[w * 32 + 16] = Mw; fin[w * 32 + 17] = L; }
    }
    __syncthreads();
    if (tid < 16) {
      float Mx = -INFINITY;
#pragma unroll
      for (int i = 0; i < NWAVE; ++i) Mx = fmaxf(Mx, fin[i * 32 + 16]);
      float Lx = 0.f, ox = 0.f;
#pragma unroll
      for (int i = 0; i < NWAVE; ++i) {
        const float ee = __builtin_amdgcn_exp2f(fin[i * 32 + 16] - Mx);
        Lx = fmaf(ee, fin[i * 32 + 17], Lx);
        ox = fmaf(ee, fin[i * 32 + tid], ox);
      }
      const __bf16 ob = (__bf16)(ox / Lx);
      out[(hk * 8 + (cb >> 3)) * DH + 16 * (cb & 7) + tid] = __builtin_bit_cast(unsigned short, ob);
      if (cb == 0 && tid == 0) __hip_atomic_store(epoch, tag, RLX_AGENT);
    }
  }
}
}

extern "C" void kernel_launch(void* const* d_in, const int* in_sizes, int n_in, void* d_out, int out_size, void* d_ws,
                              size_t ws_size, hipStream_t stream) {
  kv_decode<<<HKV * NSPLIT, NTHR, 0, stream>>>((const unsigned short*)d_in[0], (const int*)d_in[1], (const int*)d_in[2],
                                               (const unsigned short*)d_in[3], (const unsigned short*)d_in[4],
                                               (const unsigned short*)d_in[5], (const unsigned short*)d_in[6],
                                               (unsigned char*)d_ws, (unsigned short*)d_out);
}
```

```cpp
#include <hip/hip_runtime.h>
#include <hip/hip_bf16.h>
#include <stdint.h>

#define DIST 6
#define POLL_SLEEP 2
#define FETCH_MISS 8
#define BURST 16
#define LASTD 16
#define HSTAG 2
#define LDS_PAD 0

namespace {
constexpr int HKV = 4, SKV = 65536, DH = 128, GS = 128, NGRP = SKV / GS;
constexpr int NWAVE = 4, NTHR = NWAVE * 64;
constexpr int NSPLIT = 64;
constexpr int NUNIT = NGRP * 4;
constexpr int UPI = NSPLIT * NWAVE;
constexpr int NIT = NUNIT / UPI;
constexpr int OSTR = 132;
constexpr int WBYTES = 16 * OSTR * 4;
constexpr size_t WS_INBOX = 4096;
constexpr size_t WS_PAY = WS_INBOX + (size_t)HKV * NSPLIT * NSPLIT * 8;
constexpr unsigned SPIN_CAP = 1u << 20;

typedef unsigned v4u __attribute__((ext_vector_type(4)));
typedef unsigned v2u __attribute__((ext_vector_type(2)));
typedef float f32x4 __attribute__((ext_vector_type(4)));
typedef float f32x2 __attribute__((ext_vector_type(2)));
typedef __bf16 bf16x8 __attribute__((ext_vector_type(8)));
typedef __bf16 bf16x2 __attribute__((ext_vector_type(2)));
#define LAS __attribute__((address_space(3)))
#define GAS __attribute__((address_space(1)))
typedef GAS unsigned gu32;
typedef GAS unsigned long long gu64;
#define RLX_AGENT __ATOMIC_RELAXED, __HIP_MEMORY_SCOPE_AGENT
#define RSRC(p, n) __builtin_amdgcn_make_buffer_rsrc((void*)(p), 0, (n), 0x00020000)

__device__ __forceinline__ unsigned pk_bf16(float lo, float hi) {
  f32x2 v = {lo, hi};
  return __builtin_bit_cast(unsigned, __builtin_convertvector(v, bf16x2));
}
__device__ __forceinline__ float bf_lo(unsigned p) { return __uint_as_float(p << 16); }
__device__ __forceinline__ float bf_hi(unsigned p) { return __uint_as_float(p & 0xffff0000u); }
__device__ __forceinline__ f32x4 mfma16(v4u a, v4u b, f32x4 c) {
  return __builtin_amdgcn_mfma_f32_16x16x32_bf16(__builtin_bit_cast(bf16x8, a), __builtin_bit_cast(bf16x8, b), c, 0, 0, 0);
}
__device__ __forceinline__ v4u ldnt(__amdgpu_buffer_rsrc_t rs, int voff, int soff) {
  return __builtin_amdgcn_raw_buffer_load_b128(rs, voff, soff, 2);
}
__device__ __forceinline__ v2u ldnt8(__amdgpu_buffer_rsrc_t rs, int voff, int soff) {
  return __builtin_amdgcn_raw_buffer_load_b64(rs, voff, soff, 2);
}
template <int CTRL> __device__ __forceinline__ float dpp(float x) {
  return __int_as_float(__builtin_amdgcn_mov_dpp(__float_as_int(x), CTRL, 0xf, 0xf, true));
}
constexpr int ROR1 = 0x121, ROR2 = 0x122, ROR4 = 0x124, ROR8 = 0x128;
__device__ __forceinline__ float xrow_max(float x) {
  auto s = __builtin_amdgcn_permlane16_swap(__float_as_uint(x), __float_as_uint(x), false, false);
  x = fmaxf(__uint_as_float(s[0]), __uint_as_float(s[1]));
  auto t = __builtin_amdgcn_permlane32_swap(__float_as_uint(x), __float_as_uint(x), false, false);
  return fmaxf(__uint_as_float(t[0]), __uint_as_float(t[1]));
}
__device__ __forceinline__ float xrow_sum(float x) {
  auto s = __builtin_amdgcn_permlane16_swap(__float_as_uint(x), __float_as_uint(x), false, false);
  x = __uint_as_float(s[0]) + __uint_as_float(s[1]);
  auto t = __builtin_amdgcn_permlane32_swap(__float_as_uint(x), __float_as_uint(x), false, false);
  return __uint_as_float(t[0]) + __uint_as_float(t[1]);
}
__device__ __forceinline__ float row_sum(float x) {
  x += dpp<ROR1>(x);
  x += dpp<ROR2>(x);
  x += dpp<ROR4>(x);
  return x + dpp<ROR8>(x);
}

template <int N> struct IntT { static constexpr int value = N; };
struct TrueT { static constexpr bool value = true; };
struct FalseT { static constexpr bool value = false; };
constexpr float LOG2E = 1.4426950408889634f;
constexpr float SCALE = 0.08838834764831845f;

__global__ void __launch_bounds__(NTHR, 2)
kv_decode(const unsigned short* __restrict__ q, const int* __restrict__ qk, const int* __restrict__ qv,
          const unsigned short* __restrict__ ks, const unsigned short* __restrict__ kz,
          const unsigned short* __restrict__ vs, const unsigned short* __restrict__ vz,
          unsigned char* ws, unsigned short* __restrict__ out) {
  __shared__ __attribute__((aligned(16))) unsigned char smem[NWAVE * WBYTES + NWAVE * 8 * 16 + NWAVE * NIT * 64 * 8 + LDS_PAD];
  const int tid = threadIdx.x, lane = tid & 63;
  const int w = __builtin_amdgcn_readfirstlane(tid >> 6);
  const int hk = blockIdx.x / NSPLIT, cb = blockIdx.x % NSPLIT;
  const int rot = ((blockIdx.x >> 3) + 2 * w) & 7;
  constexpr int HEADB = SKV * DH * 4;
  const char* kh = (const char*)qk + (size_t)hk * HEADB;
  const __amdgpu_buffer_rsrc_t rk = RSRC(kh, HEADB);
  const __amdgpu_buffer_rsrc_t rks = RSRC(ks + (size_t)hk * NGRP * DH, NGRP * DH * 2);
  const __amdgpu_buffer_rsrc_t rkz = RSRC(kz + (size_t)hk * NGRP * DH, NGRP * DH * 2);
  const int voffK = lane * 16, voffS = 8 * (lane & 31);
#define UBASE(j) (((j) >> 1) * 32768 + ((j) & 1) * 2048)
#define LDK(rs, gb, p) ldnt(rs, voffK + ((p) & 1) * 1024, (gb) + ((((p) >> 1) ^ rot) << 12))
#define LDV(rs, gb, n) ldnt(rs, (((n) & 4) ? voffV1 : voffV0) + ((n) & 3) * 512 + ((n) >> 3) * 256, (gb))
#define SOFF_S(j) (((j) >> 2) * 256)
#define SOFF_T(j) (((j) >> 1) * 128 + ((j) & 1) * 8)
  const int j0 = (cb * NWAVE + w + HSTAG * hk * UPI) & (NUNIT - 1);
  v2u kzr = ldnt8(rkz, voffS, SOFF_S(j0));
  v2u ksr = ldnt8(rks, voffS, SOFF_S(j0));
  v4u a[16];
#pragma unroll
  for (int i = 0; i < 8; ++i) a[i] = LDK(rk, UBASE(j0), i);
  __builtin_amdgcn_sched_barrier(0);
  const int g = lane >> 4, c = lane & 15, u = (lane >> 3) & 1, h = lane & 7;
  v4u qf[4];
#pragma unroll
  for (int kk = 0; kk < 4; ++kk) qf[kk] = __builtin_nontemporal_load((const v4u*)(q + (size_t)(hk * 8 + h) * DH + 32 * kk + 8 * g));
  __builtin_amdgcn_sched_barrier(0);
#pragma unroll
  for (int i = 8; i < BURST; ++i) a[i] = LDK(rk, UBASE(j0), i);
  __builtin_amdgcn_sched_barrier(0);
  const unsigned tag = *(const volatile __attribute__((address_space(4))) unsigned*)(ws + hk * 256) + 1u;
  __builtin_amdgcn_sched_barrier(0);

  const __amdgpu_buffer_rsrc_t rv = RSRC((const char*)qv + (size_t)hk * HEADB, HEADB);
  const __amdgpu_buffer_rsrc_t rvs = RSRC(vs + (size_t)hk * SKV, SKV * 2);
  const __amdgpu_buffer_rsrc_t rvz = RSRC(vz + (size_t)hk * SKV, SKV * 2);
  const int voffV0 = ((g ^ rot) << 12) + c * 16, voffV1 = (((4 + g) ^ rot) << 12) + c * 16;
  const int voffT = 16 * ((4 * u + g) ^ rot);
  LAS unsigned char* kbuf = (LAS unsigned char*)smem + w * WBYTES;
  LAS f32x4* mbuf = (LAS f32x4*)((LAS unsigned char*)smem + NWAVE * WBYTES);
  LAS v2u* sbuf = (LAS v2u*)((LAS unsigned char*)smem + NWAVE * WBYTES + NWAVE * 8 * 16) + w * (NIT * 64) + lane;
  const int hi = lane >> 5;
  const int wr0 = hi * 256 + ((((lane & 31) >> 1) ^ hi) << 4) + 8 * (lane & 1);
  float m = -INFINITY;
  v2u vsr, vzr;
#define KSTAMP(it_) {}

  auto k_unit = [&](auto ISSUED_, auto LAST_, int j, int it) __attribute__((always_inline)) {
    constexpr int ISSUED = decltype(ISSUED_)::value;
    constexpr bool LAST = decltype(LAST_)::value;
    const int gb = UBASE(j);
    const int jn = LAST ? j0 : (j + UPI) & (NUNIT - 1);
    const int gbn = UBASE(jn);
    const float kz0 = bf_lo(kzr.x), kz1 = bf_hi(kzr.x), kz2 = bf_lo(kzr.y), kz3 = bf_hi(kzr.y);
    const float ks0 = bf_lo(ksr.x), ks1 = bf_hi(ksr.x), ks2 = bf_lo(ksr.y), ks3 = bf_hi(ksr.y);
    __builtin_amdgcn_sched_barrier(0);
#pragma unroll
    for (int p = 0; p < 16; ++p) {
      const v4u x = a[p];
      const unsigned d01 = pk_bf16((float)(int)x.x - kz0, (float)(int)x.y - kz1);
      const unsigned d23 = pk_bf16((float)(int)x.z - kz2, (float)(int)x.w - kz3);
      asm volatile("" ::"v"(d01), "v"(d23));
      if (p == 0 && !(it & 1)) KSTAMP(it >> 1)
      if (p + DIST == 16) {
        if (LAST) { vsr = ldnt8(rvs, voffT, SOFF_T(jn)); vzr = ldnt8(rvz, voffT, SOFF_T(jn)); }
        else { kzr = ldnt8(rkz, voffS, SOFF_S(jn)); ksr = ldnt8(rks, voffS, SOFF_S(jn)); }
      }
      if (p + DIST >= 16) a[(p + DIST) & 15] = LAST ? LDV(rv, gbn, (p + DIST) & 15) : LDK(rk, gbn, (p + DIST) & 15);
      else if (p + DIST >= ISSUED) a[(p + DIST) & 15] = LDK(rk, gb, (p + DIST) & 15);
      v2u kk2;
      kk2.x = pk_bf16(bf_lo(d01) * ks0, bf_hi(d01) * ks1);
      kk2.y = pk_bf16(bf_lo(d23) * ks2, bf_hi(d23) * ks3);
      *(LAS v2u*)(kbuf + (wr0 ^ (32 * (p & 7))) + 512 * p) = kk2;
      __builtin_amdgcn_sched_barrier(0);
    }
    f32x4 d0 = {0.f, 0.f, 0.f, 0.f}, d1 = {0.f, 0.f, 0.f, 0.f};
#pragma unroll
    for (int kk = 0; kk < 4; ++kk) {
      const int ra = c * 256 + (((4 * kk + g) ^ c) << 4);
      const v4u k0 = *(LAS v4u*)(kbuf + ra);
      const v4u k1 = *(LAS v4u*)(kbuf + ra + 4096);
      d0 = mfma16(k0, qf[kk], d0);
      d1 = mfma16(k1, qf[kk], d1);
    }
    const unsigned r01 = pk_bf16(u ? d1[0] : d0[0], u ? d1[1] : d0[1]);
    const unsigned r23 = pk_bf16(u ? d1[2] : d0[2], u ? d1[3] : d0[3]);
    v2u sv;
    sv.x = pk_bf16(bf_lo(r01) * SCALE, bf_hi(r01) * SCALE);
    sv.y = pk_bf16(bf_lo(r23) * SCALE, bf_hi(r23) * SCALE);
    sbuf[it * 64] = sv;
    m = fmaxf(fmaxf(m, fmaxf(bf_lo(sv.x), bf_hi(sv.x))), fmaxf(bf_lo(sv.y), bf_hi(sv.y)));
    __builtin_amdgcn_sched_barrier(0);
  };
  k_unit(IntT<BURST>{}, FalseT{}, j0, 0);
#pragma nounroll
  for (int it = 1; it < NIT - 1; ++it) k_unit(IntT<DIST>{}, FalseT{}, (j0 + it * UPI) & (NUNIT - 1), it);
  k_unit(IntT<DIST>{}, TrueT{}, (j0 + (NIT - 1) * UPI) & (NUNIT - 1), NIT - 1);

  m = fmaxf(m, dpp<ROR8>(m));
  m = xrow_max(m);
  float lsum = 0.f, csum = 0.f;
  f32x4 acc[2][4];
#pragma unroll
  for (int hf = 0; hf < 2; ++hf)
#pragma unroll
    for (int e = 0; e < 4; ++e) acc[hf][e] = f32x4{0.f, 0.f, 0.f, 0.f};
  float wgt[2][NWAVE], cterm[2], lse2 = 0.f;
  auto v_unit = [&](auto LAST_, int j, int it) __attribute__((always_inline)) {
    constexpr bool LAST = decltype(LAST_)::value;
    const int gb = UBASE(j);
    const int jn = (j + UPI) & (NUNIT - 1);
    const int gbn = UBASE(jn);
    const v2u sv = sbuf[it * 64];
    if (LAST) {
#pragma unroll
      for (int n = DIST; n < LASTD; ++n) a[n] = LDV(rv, gb, n);
    }
    __builtin_amdgcn_sched_barrier(0);
    constexpr int D = LAST ? LASTD : DIST;
    v4u pf;
#pragma unroll
    for (int hf = 0; hf < 2; ++hf) {
      v4u vf[4];
#pragma unroll
      for (int jp = 0; jp < 4; ++jp) {
        const int n = hf * 8 + 2 * jp;
        const v4u x0 = a[n], x1 = a[n + 1];
#pragma unroll
        for (int e = 0; e < 4; ++e) vf[e][jp] = pk_bf16((float)(int)x0[e], (float)(int)x1[e]);
        asm volatile("" ::"v"(vf[0][jp]), "v"(vf[1][jp]), "v"(vf[2][jp]), "v"(vf[3][jp]));
        if (n == 0 && !(it & 1)) KSTAMP(4 + (it >> 1))
        if (n + D < 16) a[(n + D) & 15] = LDV(rv, gb, (n + D) & 15);
        else if (!LAST) a[(n + D) & 15] = LDV(rv, gbn, (n + D) & 15);
        if (n + 1 + D < 16) a[(n + 1 + D) & 15] = LDV(rv, gb, (n + 1 + D) & 15);
        else if (!LAST) a[(n + 1 + D) & 15] = LDV(rv, gbn, (n + 1 + D) & 15);
        __builtin_amdgcn_sched_barrier(0);
        if (n == 0) {
          const float p0 = __builtin_amdgcn_exp2f((bf_lo(sv.x) - m) * LOG2E), p1 = __builtin_amdgcn_exp2f((bf_hi(sv.x) - m) * LOG2E);
          const float p2 = __builtin_amdgcn_exp2f((bf_lo(sv.y) - m) * LOG2E), p3 = __builtin_amdgcn_exp2f((bf_hi(sv.y) - m) * LOG2E);
          lsum += (p0 + p1) + (p2 + p3);
          const unsigned p01 = pk_bf16(p0 * bf_lo(vsr.x), p1 * bf_hi(vsr.x));
          const unsigned p23 = pk_bf16(p2 * bf_lo(vsr.y), p3 * bf_hi(vsr.y));
          csum = fmaf(bf_lo(p01), bf_lo(vzr.x), csum);
          csum = fmaf(bf_hi(p01), bf_hi(vzr.x), csum);
          csum = fmaf(bf_lo(p23), bf_lo(vzr.y), csum);
          csum = fmaf(bf_hi(p23), bf_hi(vzr.y), csum);
          pf.x = u ? 0u : p01;
          pf.y = u ? 0u : p23;
          pf.z = u ? p01 : 0u;
          pf.w = u ? p23 : 0u;
          asm volatile("" ::"v"(pf), "v"(csum), "v"(lsum));
          if (!LAST) {
            vsr = ldnt8(rvs, voffT, SOFF_T(jn));
            vzr = ldnt8(rvz, voffT, SOFF_T(jn));
          } else {
            const float lw = xrow_sum(lsum + dpp<ROR8>(lsum)), cw = xrow_sum(csum + dpp<ROR8>(csum));
            if (lane < 8) mbuf[w * 8 + h] = f32x4{m, lw, cw, 0.f};
          }
          __builtin_amdgcn_sched_barrier(0);
        }
        if (LAST && n + 2 + D >= 16 && (n == 0 || n + D < 16)) {
          __syncthreads();
#pragma unroll
          for (int k = 0; k < 2; ++k) {
            f32x4 ml[NWAVE];
#pragma unroll
            for (int i = 0; i < NWAVE; ++i) ml[i] = mbuf[i * 8 + w + NWAVE * k];
            const float M = fmaxf(fmaxf(ml[0][0], ml[1][0]), fmaxf(ml[2][0], ml[3][0]));
            float L = 0.f, C = 0.f;
#pragma unroll
            for (int i = 0; i < NWAVE; ++i) {
              wgt[k][i] = __builtin_amdgcn_exp2f((ml[i][0] - M) * LOG2E);
              L = fmaf(wgt[k][i], ml[i][1], L);
              C = fmaf(wgt[k][i], ml[i][2], C);
            }
            const float inv = __builtin_amdgcn_rcpf(L);
#pragma unroll
            for (int i = 0; i < NWAVE; ++i) wgt[k][i] *= inv;
            cterm[k] = -C * inv;
            const float lse = fmaf(M, LOG2E, __builtin_amdgcn_logf(L));
            lse2 = (k == 0 || u) ? lse : lse2;
          }
          asm volatile("" ::"v"(wgt[0][0]), "v"(wgt[0][1]), "v"(wgt[0][2]), "v"(wgt[0][3]), "v"(wgt[1][0]), "v"(wgt[1][1]), "v"(wgt[1][2]), "v"(wgt[1][3]),
                       "v"(cterm[0]), "v"(cterm[1]), "v"(lse2));
          __builtin_amdgcn_sched_barrier(0);
        }
      }
#pragma unroll
      for (int e = 0; e < 4; ++e) acc[hf][e] = mfma16(vf[e], pf, acc[hf][e]);
      __builtin_amdgcn_sched_barrier(0);
    }
  };
#pragma nounroll
  for (int it = 0; it < NIT - 1; ++it) v_unit(FalseT{}, (j0 + it * UPI) & (NUNIT - 1), it);
  v_unit(TrueT{}, (j0 + (NIT - 1) * UPI) & (NUNIT - 1), NIT - 1);
  {
    LAS float* obuf = (LAS float*)kbuf + c * OSTR + 16 * g;
#pragma unroll
    for (int hf = 0; hf < 2; ++hf)
#pragma unroll
      for (int e = 0; e < 4; ++e) *(LAS f32x4*)(obuf + 64 * hf + 4 * e) = acc[hf][e];
  }
  const __amdgpu_buffer_rsrc_t rp = RSRC(ws + WS_PAY + (size_t)hk * NSPLIT * NSPLIT * 128, NSPLIT * NSPLIT * 128);
  gu64* inbox = (gu64*)(ws + WS_INBOX) + (size_t)hk * NSPLIT * NSPLIT;
  __syncthreads();
  {
    f32x2 ob[2][NWAVE][2];
#pragma unroll
    for (int k = 0; k < 2; ++k)
#pragma unroll
      for (int i = 0; i < NWAVE; ++i) {
        const LAS float* op = (const LAS float*)((LAS unsigned char*)smem + i * WBYTES) + (w + NWAVE * k) * OSTR + 2 * lane;
        ob[k][i][0] = *(const LAS f32x2*)op;
        ob[k][i][1] = *(const LAS f32x2*)(op + 8 * OSTR);
      }
#pragma unroll
    for (int k = 0; k < 2; ++k) {
      f32x2 o = {cterm[k], cterm[k]};
#pragma unroll
      for (int i = 0; i < NWAVE; ++i) o += wgt[k][i] * (ob[k][i][0] + ob[k][i][1]);
      const v4u pk = {__float_as_uint(o[0]), tag, __float_as_uint(o[1]), tag};
      __builtin_amdgcn_raw_buffer_store_b128(pk, rp, (((w + NWAVE * k) * 8 + (lane >> 3)) * NSPLIT + cb) * 128 + (lane & 7) * 16, 0, 16);
    }
    if (lane < 16) __hip_atomic_store(inbox + ((w + NWAVE * u) * 8 + h) * NSPLIT + cb, ((unsigned long long)tag << 32) | __float_as_uint(lse2), RLX_AGENT);
  }
  {
    const int poff = (cb * NSPLIT + lane) * 128 + w * 32;
    gu64* ib = inbox + cb * NSPLIT + lane;
    float o[4] = {0.f, 0.f, 0.f, 0.f}, lse = 0.f;
    bool have = false, done = false, fetch = true;
    unsigned spins = 0;
    while (true) {
      unsigned long long f = 0;
      if (!done) f = __hip_atomic_load(ib, RLX_AGENT);
      if (!have && fetch) {
        const v4u x0 = __builtin_amdgcn_raw_buffer_load_b128(rp, poff, 0, 16);
        const v4u x1 = __builtin_amdgcn_raw_buffer_load_b128(rp, poff + 16, 0, 16);
        have = (x0.y == tag) & (x0.w == tag) & (x1.y == tag) & (x1.w == tag);
        o[0] = __uint_as_float(x0.x); o[1] = __uint_as_float(x0.z);
        o[2] = __uint_as_float(x1.x); o[3] = __uint_as_float(x1.z);
      }
      const bool seen = done || (unsigned)(f >> 32) == tag;
      if (!done) lse = __uint_as_float((unsigned)f);
      done = have && seen;
      if (__all(done)) break;
      if (++spins > SPIN_CAP) { lse = __builtin_nanf(""); break; }
      fetch = seen || __builtin_popcountll(__ballot(!seen)) <= FETCH_MISS;
      __builtin_amdgcn_s_sleep(POLL_SLEEP);
    }
    float M = fmaxf(lse, dpp<ROR1>(lse));
    M = fmaxf(M, dpp<ROR2>(M));
    M = fmaxf(M, dpp<ROR4>(M));
    M = fmaxf(M, dpp<ROR8>(M));
    M = xrow_max(M);
    const float e = __builtin_amdgcn_exp2f(lse - M);
    float t;
    {
      const float a0 = o[0] * e, a1 = o[1] * e, a2 = o[2] * e, a3 = o[3] * e;
      auto s02 = __builtin_amdgcn_permlane32_swap(__float_as_uint(a0), __float_as_uint(a2), false, false);
      auto s13 = __builtin_amdgcn_permlane32_swap(__float_as_uint(a1), __float_as_uint(a3), false, false);
      const float b0 = __uint_as_float(s02[0]) + __uint_as_float(s02[1]);
      const float b1 = __uint_as_float(s13[0]) + __uint_as_float(s13[1]);
      auto s = __builtin_amdgcn_permlane16_swap(__float_as_uint(b0), __float_as_uint(b1), false, false);
      t = __uint_as_float(s[0]) + __uint_as_float(s[1]);
    }
    t = row_sum(t);
    const float L = row_sum(xrow_sum(e));
    const unsigned ob = pk_bf16(t * __builtin_amdgcn_rcpf(L), 0.f);
    if (c == 0) out[(hk * 8 + (cb >> 3)) * DH + 16 * (cb & 7) + 4 * g + w] = (unsigned short)ob;
    if (cb == 0 && tid == 0) __hip_atomic_store((gu32*)ws + hk * 64, tag, RLX_AGENT);
  }
}
}

extern "C" void kernel_launch(void* const* d_in, const int* in_sizes, int n_in, void* d_out, int out_size, void* d_ws,
                              size_t ws_size, hipStream_t stream) {
  kv_decode<<<HKV * NSPLIT, NTHR, 0, stream>>>((const unsigned short*)d_in[0], (const int*)d_in[1], (const int*)d_in[2],
                                               (const unsigned short*)d_in[3], (const unsigned short*)d_in[4],
                                               (const unsigned short*)d_in[5], (const unsigned short*)d_in[6],
                                               (unsigned char*)d_ws, (unsigned short*)d_out);
}
```

```cpp
#include <hip/hip_runtime.h>
#include <hip/hip_bf16.h>
#include <stdint.h>

#define DK 5
#define DV 6
#define BURST 16
#define POLL_SLEEP 2
#define FETCH_MISS 4
#define PRETOUCH 0
#define PT_AUX 16
#define HSTAG 2
#define LDS_PAD 0

namespace {
constexpr int HKV = 4, SKV = 65536, DH = 128, GS = 128, NGRP = SKV / GS;
constexpr int NWAVE = 4, NTHR = NWAVE * 64;
constexpr int NSPLIT = 64;
constexpr int NUNIT = NGRP * 4;
constexpr int UPI = NSPLIT * NWAVE;
constexpr int NIT = NUNIT / UPI;
constexpr int OSTR = 132;
constexpr int WBYTES = 16 * OSTR * 4;
constexpr size_t WS_INBOX = 4096;
constexpr size_t WS_PAY = WS_INBOX + (size_t)HKV * NSPLIT * NSPLIT * 8;
constexpr unsigned SPIN_CAP = 1u << 20;

typedef unsigned v4u __attribute__((ext_vector_type(4)));
typedef unsigned v2u __attribute__((ext_vector_type(2)));
typedef float f32x4 __attribute__((ext_vector_type(4)));
typedef float f32x2 __attribute__((ext_vector_type(2)));
typedef __bf16 bf16x8 __attribute__((ext_vector_type(8)));
typedef __bf16 bf16x2 __attribute__((ext_vector_type(2)));
#define LAS __attribute__((address_space(3)))
#define GAS __attribute__((address_space(1)))
typedef GAS unsigned gu32;
typedef GAS unsigned long long gu64;
#define RLX_AGENT __ATOMIC_RELAXED, __HIP_MEMORY_SCOPE_AGENT
#define RSRC(p, n) __builtin_amdgcn_make_buffer_rsrc((void*)(p), 0, (n), 0x00020000)

__device__ __forceinline__ unsigned pk_bf16(float lo, float hi) {
  f32x2 v = {lo, hi};
  return __builtin_bit_cast(unsigned, __builtin_convertvector(v, bf16x2));
}
__device__ __forceinline__ float bf_lo(unsigned p) { return __uint_as_float(p << 16); }
__device__ __forceinline__ float bf_hi(unsigned p) { return __uint_as_float(p & 0xffff0000u); }
__device__ __forceinline__ f32x4 mfma16(v4u a, v4u b, f32x4 c) {
  return __builtin_amdgcn_mfma_f32_16x16x32_bf16(__builtin_bit_cast(bf16x8, a), __builtin_bit_cast(bf16x8, b), c, 0, 0, 0);
}
__device__ __forceinline__ v4u ldnt(__amdgpu_buffer_rsrc_t rs, int voff, int soff) {
  return __builtin_amdgcn_raw_buffer_load_b128(rs, voff, soff, 2);
}
__device__ __forceinline__ v2u ldnt8(__amdgpu_buffer_rsrc_t rs, int voff, int soff) {
  return __builtin_amdgcn_raw_buffer_load_b64(rs, voff, soff, 2);
}
template <int CTRL> __device__ __forceinline__ float dpp(float x) {
  return __int_as_float(__builtin_amdgcn_mov_dpp(__float_as_int(x), CTRL, 0xf, 0xf, true));
}
constexpr int ROR1 = 0x121, ROR2 = 0x122, ROR4 = 0x124, ROR8 = 0x128;
__device__ __forceinline__ float xrow_max(float x) {
  auto s = __builtin_amdgcn_permlane16_swap(__float_as_uint(x), __float_as_uint(x), false, false);
  x = fmaxf(__uint_as_float(s[0]), __uint_as_float(s[1]));
  auto t = __builtin_amdgcn_permlane32_swap(__float_as_uint(x), __float_as_uint(x), false, false);
  return fmaxf(__uint_as_float(t[0]), __uint_as_float(t[1]));
}
__device__ __forceinline__ float xrow_sum(float x) {
  auto s = __builtin_amdgcn_permlane16_swap(__float_as_uint(x), __float_as_uint(x), false, false);
  x = __uint_as_float(s[0]) + __uint_as_float(s[1]);
  auto t = __builtin_amdgcn_permlane32_swap(__float_as_uint(x), __float_as_uint(x), false, false);
  return __uint_as_float(t[0]) + __uint_as_float(t[1]);
}
__device__ __forceinline__ float row_sum(float x) {
  x += dpp<ROR1>(x);
  x += dpp<ROR2>(x);
  x += dpp<ROR4>(x);
  return x + dpp<ROR8>(x);
}

template <int N> struct IntT { static constexpr int value = N; };
struct TrueT { static constexpr bool value = true; };
struct FalseT { static constexpr bool value = false; };
constexpr float LOG2E = 1.4426950408889634f;
constexpr float SCALE = 0.08838834764831845f;

__global__ void __launch_bounds__(NTHR, 2)
kv_decode(const unsigned short* __restrict__ q, const int* __restrict__ qk, const int* __restrict__ qv,
          const unsigned short* __restrict__ ks, const unsigned short* __restrict__ kz,
          const unsigned short* __restrict__ vs, const unsigned short* __restrict__ vz,
          unsigned char* ws, unsigned short* __restrict__ out) {
  __shared__ __attribute__((aligned(16))) unsigned char smem[NWAVE * WBYTES + NWAVE * 8 * 16 + NWAVE * NIT * 64 * 8 + LDS_PAD];
  const int tid = threadIdx.x, lane = tid & 63;
  const int w = __builtin_amdgcn_readfirstlane(tid >> 6);
  const int hk = blockIdx.x / NSPLIT, cb = blockIdx.x % NSPLIT;
  const int rot = ((blockIdx.x >> 3) + 2 * w) & 7;
  constexpr int HEADB = SKV * DH * 4;
  const char* kh = (const char*)qk + (size_t)hk * HEADB;
  const __amdgpu_buffer_rsrc_t rk = RSRC(kh, HEADB);
  const __amdgpu_buffer_rsrc_t rks = RSRC(ks + (size_t)hk * NGRP * DH, NGRP * DH * 2);
  const __amdgpu_buffer_rsrc_t rkz = RSRC(kz + (size_t)hk * NGRP * DH, NGRP * DH * 2);
  const int voffK = lane * 16, voffS = 8 * (lane & 31);
#define UBASE(j) (((j) >> 1) * 32768 + ((j) & 1) * 2048)
#define LDK(rs, gb, p) ldnt(rs, voffK + ((p) & 1) * 1024, (gb) + ((((p) >> 1) ^ rot) << 12))
#define LDV(rs, gb, n) ldnt(rs, (((n) & 4) ? voffV1 : voffV0) + ((n) & 3) * 512 + ((n) >> 3) * 256, (gb))
#define SOFF_S(j) (((j) >> 2) * 256)
#define SOFF_T(j) (((j) >> 1) * 128 + ((j) & 1) * 8)
  const int j0 = (cb * NWAVE + w + HSTAG * hk * UPI) & (NUNIT - 1);
  v2u kzr = ldnt8(rkz, voffS, SOFF_S(j0));
  v2u ksr = ldnt8(rks, voffS, SOFF_S(j0));
  v4u a[16];
#pragma unroll
  for (int i = 0; i < 8; ++i) a[i] = LDK(rk, UBASE(j0), i);
  __builtin_amdgcn_sched_barrier(0);
  const int g = lane >> 4, c = lane & 15, u = (lane >> 3) & 1, h = lane & 7;
  v4u qf[4];
#pragma unroll
  for (int kk = 0; kk < 4; ++kk) qf[kk] = __builtin_nontemporal_load((const v4u*)(q + (size_t)(hk * 8 + h) * DH + 32 * kk + 8 * g));
  __builtin_amdgcn_sched_barrier(0);
#pragma unroll
  for (int i = 8; i < BURST; ++i) a[i] = LDK(rk, UBASE(j0), i);
  __builtin_amdgcn_sched_barrier(0);
  const __amdgpu_buffer_rsrc_t rp = RSRC(ws + WS_PAY + (size_t)hk * NSPLIT * NSPLIT * 64, NSPLIT * NSPLIT * 64);
  const __amdgpu_buffer_rsrc_t ri = RSRC(ws + WS_INBOX + (size_t)hk * NSPLIT * NSPLIT * 8, NSPLIT * NSPLIT * 8);
  unsigned tag = *(const volatile __attribute__((address_space(4))) unsigned*)(ws + hk * 256) + 0x01010101u;
  tag = tag ? tag : 0x01010101u;
  __builtin_amdgcn_sched_barrier(0);

  const __amdgpu_buffer_rsrc_t rv = RSRC((const char*)qv + (size_t)hk * HEADB, HEADB);
  const __amdgpu_buffer_rsrc_t rvs = RSRC(vs + (size_t)hk * SKV, SKV * 2);
  const __amdgpu_buffer_rsrc_t rvz = RSRC(vz + (size_t)hk * SKV, SKV * 2);
  const int voffV0 = ((g ^ rot) << 12) + c * 16, voffV1 = (((4 + g) ^ rot) << 12) + c * 16;
  const int voffT = 16 * ((4 * u + g) ^ rot);
  LAS unsigned char* kbuf = (LAS unsigned char*)smem + w * WBYTES;
  LAS f32x4* mbuf = (LAS f32x4*)((LAS unsigned char*)smem + NWAVE * WBYTES);
  LAS v2u* sbuf = (LAS v2u*)((LAS unsigned char*)smem + NWAVE * WBYTES + NWAVE * 8 * 16) + w * (NIT * 64) + lane;
  const int hi = lane >> 5;
  const int wr0 = hi * 256 + ((((lane & 31) >> 1) ^ hi) << 4) + 8 * (lane & 1);
  float m = -INFINITY;
  v2u vsr, vzr;
#define KSTAMP(it_) {}

#define SB() __builtin_amdgcn_sched_barrier(0)
  f32x4 d0, d1;
  v4u kf[4];
  auto frag_read = [&](int half) __attribute__((always_inline)) {
#pragma unroll
    for (int kk = 0; kk < 4; ++kk) kf[kk] = *(LAS v4u*)(kbuf + c * 256 + (((4 * kk + g) ^ c) << 4) + 4096 * half);
  };
  auto qk_mfma = [&]() __attribute__((always_inline)) {
    f32x4 d = {0.f, 0.f, 0.f, 0.f};
#pragma unroll
    for (int kk = 0; kk < 4; ++kk) d = mfma16(kf[kk], qf[kk], d);
    return d;
  };
  auto put_logits = [&](int it) __attribute__((always_inline)) {
    const unsigned r01 = pk_bf16(u ? d1[0] : d0[0], u ? d1[1] : d0[1]);
    const unsigned r23 = pk_bf16(u ? d1[2] : d0[2], u ? d1[3] : d0[3]);
    v2u sv;
    sv.x = pk_bf16(bf_lo(r01) * SCALE, bf_hi(r01) * SCALE);
    sv.y = pk_bf16(bf_lo(r23) * SCALE, bf_hi(r23) * SCALE);
    sbuf[it * 64] = sv;
    m = fmaxf(fmaxf(m, fmaxf(bf_lo(sv.x), bf_hi(sv.x))), fmaxf(bf_lo(sv.y), bf_hi(sv.y)));
    asm volatile("" ::"v"(m));
  };
  auto k_unit = [&](auto FIRST_, auto LAST_, int j, int it) __attribute__((always_inline)) {
    constexpr bool FIRST = decltype(FIRST_)::value, LAST = decltype(LAST_)::value;
    constexpr int ISSUED = FIRST ? BURST : DK;
    const int gb = UBASE(j);
    const int jn = LAST ? j0 : (j + UPI) & (NUNIT - 1);
    const int gbn = UBASE(jn);
    const float kz0 = bf_lo(kzr.x), kz1 = bf_hi(kzr.x), kz2 = bf_lo(kzr.y), kz3 = bf_hi(kzr.y);
    const float ks0 = bf_lo(ksr.x), ks1 = bf_hi(ksr.x), ks2 = bf_lo(ksr.y), ks3 = bf_hi(ksr.y);
    SB();
#pragma unroll
    for (int p = 0; p < 16; ++p) {
      const v4u x = a[p];
      asm volatile("" ::"v"(x));
      SB();
      if (p + DK < 16) { if (p + DK >= ISSUED) a[p + DK] = LDK(rk, gb, p + DK); }
      else if (!LAST) a[(p + DK) & 15] = LDK(rk, gbn, (p + DK) & 15);
      else if (p + DK - 16 < DV) a[(p + DK) & 15] = LDV(rv, gbn, (p + DK) & 15);
      if (p == 4) {
        if (LAST) { vsr = ldnt8(rvs, voffT, SOFF_T(jn)); vzr = ldnt8(rvz, voffT, SOFF_T(jn)); }
        else { kzr = ldnt8(rkz, voffS, SOFF_S(jn)); ksr = ldnt8(rks, voffS, SOFF_S(jn)); }
      }
      SB();
      if (p == 0 && !(it & 1)) KSTAMP(it >> 1)
      if (!FIRST && p == 0) frag_read(1);
      if (p == 8) frag_read(0);
      const unsigned d01 = pk_bf16((float)(int)x.x - kz0, (float)(int)x.y - kz1);
      const unsigned d23 = pk_bf16((float)(int)x.z - kz2, (float)(int)x.w - kz3);
      v2u kk2;
      kk2.x = pk_bf16(bf_lo(d01) * ks0, bf_hi(d01) * ks1);
      kk2.y = pk_bf16(bf_lo(d23) * ks2, bf_hi(d23) * ks3);
      *(LAS v2u*)(kbuf + (wr0 ^ (32 * (p & 7))) + 512 * p) = kk2;
      if (!FIRST && p == 1) d1 = qk_mfma();
      if (!FIRST && p == 2) put_logits(it - 1);
      if (p == 9) d0 = qk_mfma();
      SB();
    }
  };
  k_unit(TrueT{}, FalseT{}, j0, 0);
#pragma nounroll
  for (int it = 1; it < NIT - 1; ++it) k_unit(FalseT{}, FalseT{}, (j0 + it * UPI) & (NUNIT - 1), it);
  k_unit(FalseT{}, TrueT{}, (j0 + (NIT - 1) * UPI) & (NUNIT - 1), NIT - 1);
#pragma unroll
  for (int n = DK; n < DV; ++n) a[n] = LDV(rv, UBASE(j0), n);
  SB();
  frag_read(1);
  d1 = qk_mfma();
  put_logits(NIT - 1);

  m = fmaxf(m, dpp<ROR8>(m));
  m = xrow_max(m);
  float lsum = 0.f, csum = 0.f;
  f32x4 acc[2][4];
#pragma unroll
  for (int hf = 0; hf < 2; ++hf)
#pragma unroll
    for (int e = 0; e < 4; ++e) acc[hf][e] = f32x4{0.f, 0.f, 0.f, 0.f};
  float wgt[NWAVE], cterm, lse2;
  auto v_unit = [&](auto LAST_, int j, int it) __attribute__((always_inline)) {
    constexpr bool LAST = decltype(LAST_)::value;
    const int gb = UBASE(j);
    const int jn = (j + UPI) & (NUNIT - 1);
    const int gbn = UBASE(jn);
    if (LAST) {
#pragma unroll
      for (int n = DV; n < 16; ++n) a[n] = LDV(rv, gb, n);
    }
    const v2u sv = sbuf[it * 64];
    SB();
    v4u pf, vf[4];
    float p0, p1, p2, p3;
#pragma unroll
    for (int n = 0; n < 16; ++n) {
      if (!LAST) {
        asm volatile("" ::"v"(a[n]));
        SB();
        a[(n + DV) & 15] = LDV(rv, n + DV < 16 ? gb : gbn, (n + DV) & 15);
        SB();
      }
      if (n == 0 && !(it & 1)) KSTAMP(4 + (it >> 1))
      if (n & 1) {
#pragma unroll
        for (int e = 0; e < 4; ++e) vf[e][(n >> 1) & 3] = pk_bf16((float)(int)a[n - 1][e], (float)(int)a[n][e]);
        asm volatile("" ::"v"(vf[0][(n >> 1) & 3]), "v"(vf[1][(n >> 1) & 3]), "v"(vf[2][(n >> 1) & 3]), "v"(vf[3][(n >> 1) & 3]));
      }
      if (n == 0) {
        p0 = __builtin_amdgcn_exp2f((bf_lo(sv.x) - m) * LOG2E); p1 = __builtin_amdgcn_exp2f((bf_hi(sv.x) - m) * LOG2E);
        p2 = __builtin_amdgcn_exp2f((bf_lo(sv.y) - m) * LOG2E); p3 = __builtin_amdgcn_exp2f((bf_hi(sv.y) - m) * LOG2E);
        lsum += (p0 + p1) + (p2 + p3);
        asm volatile("" ::"v"(lsum));
      }
      if (n == (LAST ? 0 : 2)) {
        const unsigned p01 = pk_bf16(p0 * bf_lo(vsr.x), p1 * bf_hi(vsr.x));
        const unsigned p23 = pk_bf16(p2 * bf_lo(vsr.y), p3 * bf_hi(vsr.y));
        csum = fmaf(bf_lo(p01), bf_lo(vzr.x), csum);
        csum = fmaf(bf_hi(p01), bf_hi(vzr.x), csum);
        csum = fmaf(bf_lo(p23), bf_lo(vzr.y), csum);
        csum = fmaf(bf_hi(p23), bf_hi(vzr.y), csum);
        pf.x = u ? 0u : p01;
        pf.y = u ? 0u : p23;
        pf.z = u ? p01 : 0u;
        pf.w = u ? p23 : 0u;
        asm volatile("" ::"v"(pf), "v"(csum));
      }
      if (!LAST && n == 4) {
        vsr = ldnt8(rvs, voffT, SOFF_T(jn));
        vzr = ldnt8(rvz, voffT, SOFF_T(jn));
      }
      if (LAST && n == 0) {
        const float lw = xrow_sum(lsum + dpp<ROR8>(lsum)), cw = xrow_sum(csum + dpp<ROR8>(csum));
        if (lane < 8) mbuf[w * 8 + h] = f32x4{m, lw, cw, 0.f};
        __syncthreads();
        f32x4 ml[NWAVE];
#pragma unroll
        for (int i = 0; i < NWAVE; ++i) ml[i] = mbuf[i * 8 + 2 * w + hi];
        const float M = fmaxf(fmaxf(ml[0][0], ml[1][0]), fmaxf(ml[2][0], ml[3][0]));
        float L = 0.f, C = 0.f;
#pragma unroll
        for (int i = 0; i < NWAVE; ++i) {
          wgt[i] = __builtin_amdgcn_exp2f((ml[i][0] - M) * LOG2E);
          L = fmaf(wgt[i], ml[i][1], L);
          C = fmaf(wgt[i], ml[i][2], C);
        }
        const float inv = __builtin_amdgcn_rcpf(L);
#pragma unroll
        for (int i = 0; i < NWAVE; ++i) wgt[i] *= inv;
        cterm = -C * inv;
        lse2 = fmaf(M, LOG2E, __builtin_amdgcn_logf(L));
        asm volatile("" ::"v"(wgt[0]), "v"(wgt[1]), "v"(wgt[2]), "v"(wgt[3]), "v"(cterm), "v"(lse2));
      }
      if ((n & 7) == 7) {
#pragma unroll
        for (int e = 0; e < 4; ++e) acc[n >> 3][e] = mfma16(vf[e], pf, acc[n >> 3][e]);
      }
      SB();
    }
  };
#pragma nounroll
  for (int it = 0; it < NIT - 1; ++it) v_unit(FalseT{}, (j0 + it * UPI) & (NUNIT - 1), it);
  v_unit(TrueT{}, (j0 + (NIT - 1) * UPI) & (NUNIT - 1), NIT - 1);
  {
    LAS float* obuf = (LAS float*)kbuf + c * OSTR + 16 * g;
#pragma unroll
    for (int hf = 0; hf < 2; ++hf)
#pragma unroll
      for (int e = 0; e < 4; ++e) *(LAS f32x4*)(obuf + 64 * hf + 4 * e) = acc[hf][e];
  }
  __syncthreads();
  {
    f32x4 ob[NWAVE][2];
#pragma unroll
    for (int i = 0; i < NWAVE; ++i) {
      const LAS float* op = (const LAS float*)((LAS unsigned char*)smem + i * WBYTES) + (2 * w + hi) * OSTR + 4 * (lane & 31);
      ob[i][0] = *(const LAS f32x4*)op;
      ob[i][1] = *(const LAS f32x4*)(op + 8 * OSTR);
    }
    f32x4 o = {cterm, cterm, cterm, cterm};
#pragma unroll
    for (int i = 0; i < NWAVE; ++i) o += wgt[i] * (ob[i][0] + ob[i][1]);
    v4u pk;
#pragma unroll
    for (int i = 0; i < 4; ++i) pk[i] = (__float_as_uint(o[i]) & 0xffffff00u) | ((tag >> (8 * i)) & 0xffu);
    const int cons = 16 * w + (lane >> 2);
    __builtin_amdgcn_raw_buffer_store_b128(pk, rp, (cons * NSPLIT + cb) * 64 + (lane & 3) * 16, 0, 16);
    __builtin_amdgcn_raw_buffer_store_b64(v2u{__float_as_uint(lse2), tag}, ri, (lane & 3) == 0 ? (cons * NSPLIT + cb) * 8 : 0x40000000, 0, 16);
  }
  {
    const int pr = 16 * w + (lane >> 2);
    constexpr int OOB = 0x40000000;
    int po = (cb * NSPLIT + pr) * 64 + (lane & 3) * 16;
    int io = (cb * NSPLIT + pr) * 8;
    int pq = po;
    v4u xo = {0u, 0u, 0u, 0u};
    float lse = 0.f;
    unsigned spins = 0;
    while (true) {
      asm volatile("" ::: "memory");
      const v4u x = __builtin_amdgcn_raw_buffer_load_b128(rp, pq, 0, 16);
      const v2u f = __builtin_amdgcn_raw_buffer_load_b64(ri, io, 0, 16);
      const unsigned got = (x.x & 0xffu) | ((x.y & 0xffu) << 8) | ((x.z & 0xffu) << 16) | (x.w << 24);
      unsigned long long okm = __ballot(got == tag);
      okm &= okm >> 1;
      okm &= okm >> 2;
      okm = (okm & 0x1111111111111111ull) * 15ull;
      const bool k = (okm >> lane) & 1ull;
      const bool sn = f.y == tag;
      xo = k ? x : xo;
      lse = sn ? __uint_as_float(f.x) : lse;
      po = k ? OOB : po;
      const int miss = __builtin_popcountll(__ballot(!sn & (io != OOB)));
      io = (sn & (po == OOB)) ? OOB : io;
      if (__all(io == OOB)) break;
      if (++spins > SPIN_CAP) { lse = __builtin_nanf(""); break; }
      pq = (sn | (miss <= 4 * FETCH_MISS)) ? po : OOB;
      if (POLL_SLEEP) __builtin_amdgcn_s_sleep(POLL_SLEEP);
    }
    float M = fmaxf(lse, dpp<ROR4>(lse));
    M = fmaxf(M, dpp<ROR8>(M));
    M = xrow_max(M);
    const float e = __builtin_amdgcn_exp2f(lse - M);
    float t;
    {
      float av[4];
#pragma unroll
      for (int i = 0; i < 4; ++i) av[i] = e * __uint_as_float((xo[i] & 0xffffff00u) | 0x80u);
      auto s02 = __builtin_amdgcn_permlane32_swap(__float_as_uint(av[0]), __float_as_uint(av[2]), false, false);
      auto s13 = __builtin_amdgcn_permlane32_swap(__float_as_uint(av[1]), __float_as_uint(av[3]), false, false);
      const float b0 = __uint_as_float(s02[0]) + __uint_as_float(s02[1]);
      const float b1 = __uint_as_float(s13[0]) + __uint_as_float(s13[1]);
      auto sw = __builtin_amdgcn_permlane16_swap(__float_as_uint(b0), __float_as_uint(b1), false, false);
      t = __uint_as_float(sw[0]) + __uint_as_float(sw[1]);
    }
    t += dpp<ROR4>(t);
    t += dpp<ROR8>(t);
    float L = e + dpp<ROR4>(e);
    L = xrow_sum(L + dpp<ROR8>(L));
    LAS f32x4* fin = (LAS f32x4*)((LAS unsigned char*)smem + NWAVE * WBYTES + NWAVE * 8 * 16);
    if (c < 4) fin[w * 16 + 4 * g + c] = f32x4{t, M, L, 0.f};
    __syncthreads();
    if (tid < 16) {
      f32x4 r[NWAVE];
#pragma unroll
      for (int i = 0; i < NWAVE; ++i) r[i] = fin[i * 16 + tid];
      const float Mx = fmaxf(fmaxf(r[0][1], r[1][1]), fmaxf(r[2][1], r[3][1]));
      float Lx = 0.f, ox = 0.f;
#pragma unroll
      for (int i = 0; i < NWAVE; ++i) {
        const float ee = __builtin_amdgcn_exp2f(r[i][1] - Mx);
        Lx = fmaf(ee, r[i][2], Lx);
        ox = fmaf(ee, r[i][0], ox);
      }
      out[(hk * 8 + (cb >> 3)) * DH + 16 * (cb & 7) + tid] = (unsigned short)pk_bf16(ox * __builtin_amdgcn_rcpf(Lx), 0.f);
      if (cb == 0 && tid == 0) __hip_atomic_store((gu32*)ws + hk * 64, tag, RLX_AGENT);
    }
  }
}
}

extern "C" void kernel_launch(void* const* d_in, const int* in_sizes, int n_in, void* d_out, int out_size, void* d_ws,
                              size_t ws_size, hipStream_t stream) {
  kv_decode<<<HKV * NSPLIT, NTHR, 0, stream>>>((const unsigned short*)d_in[0], (const int*)d_in[1], (const int*)d_in[2],
                                               (const unsigned short*)d_in[3], (const unsigned short*)d_in[4],
                                               (const unsigned short*)d_in[5], (const unsigned short*)d_in[6],
                                               (unsigned char*)d_ws, (unsigned short*)d_out);
}
```

```cpp
#define T_UNIT1 395
#define TAU 218
#include <hip/hip_runtime.h>
#include <hip/hip_bf16.h>
#include <stdint.h>

#define DK 6
#define DV 6
#define BURST 16
#define POLL_SLEEP 2
#define FETCH_MISS 4
#define SWZ 1
#define FIRST_SLEEP 8
#define ONEW 0
#define EARLYW 1
#define KFIRST 4
#define SLOW_SLEEP 4
#define CAP 5
#define CAP2 0
#define LATE2 50
#define CHK 4
#define HSTAG 2
#define LDS_PAD 0

namespace {
constexpr int HKV = 4, SKV = 65536, DH = 128, GS = 128, NGRP = SKV / GS;
constexpr int NWAVE = 4, NTHR = NWAVE * 64;
constexpr int NSPLIT = 64;
constexpr int NUNIT = NGRP * 4;
constexpr int UPI = NSPLIT * NWAVE;
constexpr int NIT = NUNIT / UPI;
constexpr int OSTR = 132;
constexpr int WBYTES = 16 * OSTR * 4;
constexpr size_t WS_PAY = 4096;
constexpr unsigned SPIN_CAP = 1u << 20;

typedef unsigned v4u __attribute__((ext_vector_type(4)));
typedef unsigned v2u __attribute__((ext_vector_type(2)));
typedef float f32x4 __attribute__((ext_vector_type(4)));
typedef float f32x2 __attribute__((ext_vector_type(2)));
typedef __bf16 bf16x8 __attribute__((ext_vector_type(8)));
typedef __bf16 bf16x2 __attribute__((ext_vector_type(2)));
#define LAS __attribute__((address_space(3)))
#define GAS __attribute__((address_space(1)))
typedef GAS unsigned gu32;
typedef GAS unsigned long long gu64;
#define RLX_AGENT __ATOMIC_RELAXED, __HIP_MEMORY_SCOPE_AGENT
#define RSRC(p, n) __builtin_amdgcn_make_buffer_rsrc((void*)(p), 0, (n), 0x00020000)

__device__ __forceinline__ unsigned pk_bf16(float lo, float hi) {
  f32x2 v = {lo, hi};
  return __builtin_bit_cast(unsigned, __builtin_convertvector(v, bf16x2));
}
__device__ __forceinline__ float bf_lo(unsigned p) { return __uint_as_float(p << 16); }
__device__ __forceinline__ float bf_hi(unsigned p) { return __uint_as_float(p & 0xffff0000u); }
__device__ __forceinline__ f32x4 mfma16(v4u a, v4u b, f32x4 c) {
  return __builtin_amdgcn_mfma_f32_16x16x32_bf16(__builtin_bit_cast(bf16x8, a), __builtin_bit_cast(bf16x8, b), c, 0, 0, 0);
}
__device__ __forceinline__ v4u ldnt(__amdgpu_buffer_rsrc_t rs, int voff, int soff) {
  return __builtin_amdgcn_raw_buffer_load_b128(rs, voff, soff, 2);
}
__device__ __forceinline__ v2u ldnt8(__amdgpu_buffer_rsrc_t rs, int voff, int soff) {
  return __builtin_amdgcn_raw_buffer_load_b64(rs, voff, soff, 2);
}
template <int CTRL> __device__ __forceinline__ float dpp(float x) {
  return __int_as_float(__builtin_amdgcn_mov_dpp(__float_as_int(x), CTRL, 0xf, 0xf, true));
}
constexpr int ROR1 = 0x121, ROR2 = 0x122, ROR4 = 0x124, ROR8 = 0x128;
__device__ __forceinline__ float xrow_max(float x) {
  auto s = __builtin_amdgcn_permlane16_swap(__float_as_uint(x), __float_as_uint(x), false, false);
  x = fmaxf(__uint_as_float(s[0]), __uint_as_float(s[1]));
  auto t = __builtin_amdgcn_permlane32_swap(__float_as_uint(x), __float_as_uint(x), false, false);
  return fmaxf(__uint_as_float(t[0]), __uint_as_float(t[1]));
}
__device__ __forceinline__ float xrow_sum(float x) {
  auto s = __builtin_amdgcn_permlane16_swap(__float_as_uint(x), __float_as_uint(x), false, false);
  x = __uint_as_float(s[0]) + __uint_as_float(s[1]);
  auto t = __builtin_amdgcn_permlane32_swap(__float_as_uint(x), __float_as_uint(x), false, false);
  return __uint_as_float(t[0]) + __uint_as_float(t[1]);
}
__device__ __forceinline__ float row_sum(float x) {
  x += dpp<ROR1>(x);
  x += dpp<ROR2>(x);
  x += dpp<ROR4>(x);
  return x + dpp<ROR8>(x);
}

template <int N> struct IntT { static constexpr int value = N; };
struct TrueT { static constexpr bool value = true; };
struct FalseT { static constexpr bool value = false; };
constexpr float LOG2E = 1.4426950408889634f;
constexpr float SCALE = 0.08838834764831845f;

__global__ void __launch_bounds__(NTHR, 2)
kv_decode(const unsigned short* __restrict__ q, const int* __restrict__ qk, const int* __restrict__ qv,
          const unsigned short* __restrict__ ks, const unsigned short* __restrict__ kz,
          const unsigned short* __restrict__ vs, const unsigned short* __restrict__ vz,
          unsigned char* ws, unsigned short* __restrict__ out) {
  __shared__ __attribute__((aligned(16))) unsigned char smem[NWAVE * WBYTES + NWAVE * 8 * 16 + NWAVE * NIT * 64 * 8 + LDS_PAD];
  asm volatile("" ::"s"(qk), "s"(kz), "s"(out));
  const int tid = threadIdx.x, lane = tid & 63;
  const int w = __builtin_amdgcn_readfirstlane(tid >> 6);
  const int hk = blockIdx.x / NSPLIT, cb = blockIdx.x % NSPLIT;
  const int rot = ((blockIdx.x >> 3) + 2 * w) & 7;
  constexpr int HEADB = SKV * DH * 4;
  const char* kh = (const char*)qk + (size_t)hk * HEADB;
  const __amdgpu_buffer_rsrc_t rk = RSRC(kh, HEADB);
  const int voffK = lane * 16;
#define UBASE(j) (((j) >> 1) * 32768 + ((j) & 1) * 2048)
#define LDK(rs, gb, p) ldnt(rs, voffK + ((p) & 1) * 1024, (gb) + ((((p) >> 1) ^ rot) << 12))
#define LDV(rs, gb, n) ldnt(rs, (((n) & 4) ? voffV1 : voffV0) + ((n) & 3) * 512 + ((n) >> 3) * 256, (gb))
#define SOFF_S(j) (((j) >> 2) * 256)
#define SOFF_T(j) (((j) >> 1) * 128 + ((j) & 1) * 8)
  const int j0 = (cb * NWAVE + w + HSTAG * hk * UPI) & (NUNIT - 1);
  v4u a[16];
#pragma unroll
  for (int i = 0; i < KFIRST; ++i) a[i] = LDK(rk, UBASE(j0), i);
  __builtin_amdgcn_sched_barrier(0);
  unsigned due = (unsigned)__builtin_amdgcn_s_memrealtime() + T_UNIT1;
  const __amdgpu_buffer_rsrc_t rks = RSRC(ks + (size_t)hk * NGRP * DH, NGRP * DH * 2);
  const __amdgpu_buffer_rsrc_t rkz = RSRC(kz + (size_t)hk * NGRP * DH, NGRP * DH * 2);
  const int voffS = 8 * (lane & 31);
  v2u kzr = ldnt8(rkz, voffS, SOFF_S(j0));
  v2u ksr = ldnt8(rks, voffS, SOFF_S(j0));
#pragma unroll
  for (int i = KFIRST; i < 8; ++i) a[i] = LDK(rk, UBASE(j0), i);
  __builtin_amdgcn_sched_barrier(0);
  const int g = lane >> 4, c = lane & 15, u = (lane >> 3) & 1, h = lane & 7;
  v4u qf[4];
#pragma unroll
  for (int kk = 0; kk < 4; ++kk) qf[kk] = __builtin_nontemporal_load((const v4u*)(q + (size_t)(hk * 8 + h) * DH + 32 * kk + 8 * g));
  __builtin_amdgcn_sched_barrier(0);
#pragma unroll
  for (int i = 8; i < BURST; ++i) a[i] = LDK(rk, UBASE(j0), i);
  __builtin_amdgcn_sched_barrier(0);
  const __amdgpu_buffer_rsrc_t rp = RSRC(ws + WS_PAY + (size_t)hk * NSPLIT * NSPLIT * 64, NSPLIT * NSPLIT * 64);
  unsigned tag = *(const volatile __attribute__((address_space(4))) unsigned*)(ws + hk * 256) + 0x01010101u;
  tag = tag ? tag : 0x01010101u;
  __builtin_amdgcn_sched_barrier(0);

  const __amdgpu_buffer_rsrc_t rv = RSRC((const char*)qv + (size_t)hk * HEADB, HEADB);
  const __amdgpu_buffer_rsrc_t rvs = RSRC(vs + (size_t)hk * SKV, SKV * 2);
  const __amdgpu_buffer_rsrc_t rvz = RSRC(vz + (size_t)hk * SKV, SKV * 2);
  const int voffV0 = ((g ^ rot) << 12) + c * 16, voffV1 = (((4 + g) ^ rot) << 12) + c * 16;
  const int voffT = 16 * ((4 * u + g) ^ rot);
  LAS unsigned char* kbuf = (LAS unsigned char*)smem + w * WBYTES;
  LAS f32x4* mbuf = (LAS f32x4*)((LAS unsigned char*)smem + NWAVE * WBYTES);
  LAS v2u* sbuf = (LAS v2u*)((LAS unsigned char*)smem + NWAVE * WBYTES + NWAVE * 8 * 16) + w * (NIT * 64) + lane;
  const int hi = lane >> 5;
  const int wr0 = hi * 256 + ((((lane & 31) >> 1) ^ hi) << 4) + 8 * (lane & 1);
  float m = -INFINITY;
  v2u vsr, vzr;
#define KSTAMP(it_) {}

#define SB() __builtin_amdgcn_sched_barrier(0)
  int ahead = 1;
#define WAITN(c, step) ((c) - 1 + (((step) > 4 && (step) <= 4 + (c)) ? 2 : 0))
#define THROTTLE(step) asm volatile("s_cmp_lt_i32 %0, 0\n\ts_cbranch_scc1 1f\n\ts_waitcnt vmcnt(%1)\n1:" ::"s"(ahead), "i"(WAITN(CAP, step)) : "scc")
#define SCHED(step) do { if ((step) % CHK == 0) { ahead = (int)(due + (step) * TAU / 16 - (unsigned)__builtin_amdgcn_s_memrealtime()); if ((step) + CHK >= 16) due += TAU; } } while (0)
  f32x4 d0, d1;
  v4u kf[4];
  auto frag_read = [&](int half) __attribute__((always_inline)) {
#pragma unroll
    for (int kk = 0; kk < 4; ++kk) kf[kk] = *(LAS v4u*)(kbuf + c * 256 + (((4 * kk + g) ^ c) << 4) + 4096 * half);
  };
  auto qk_mfma = [&]() __attribute__((always_inline)) {
    f32x4 d = {0.f, 0.f, 0.f, 0.f};
#pragma unroll
    for (int kk = 0; kk < 4; ++kk) d = mfma16(kf[kk], qf[kk], d);
    return d;
  };
  auto put_logits = [&](int it) __attribute__((always_inline)) {
    const unsigned r01 = pk_bf16(u ? d1[0] : d0[0], u ? d1[1] : d0[1]);
    const unsigned r23 = pk_bf16(u ? d1[2] : d0[2], u ? d1[3] : d0[3]);
    v2u sv;
    sv.x = pk_bf16(bf_lo(r01) * SCALE, bf_hi(r01) * SCALE);
    sv.y = pk_bf16(bf_lo(r23) * SCALE, bf_hi(r23) * SCALE);
    sbuf[it * 64] = sv;
    m = fmaxf(fmaxf(m, fmaxf(bf_lo(sv.x), bf_hi(sv.x))), fmaxf(bf_lo(sv.y), bf_hi(sv.y)));
    asm volatile("" ::"v"(m));
  };
  auto k_unit = [&](auto FIRST_, auto LAST_, int j, int it) __attribute__((always_inline)) {
    constexpr bool FIRST = decltype(FIRST_)::value, LAST = decltype(LAST_)::value;
    constexpr int ISSUED = FIRST ? BURST : DK;
    const int gb = UBASE(j);
    const int jn = LAST ? j0 : (j + UPI) & (NUNIT - 1);
    const int gbn = UBASE(jn);
    const float kz0 = bf_lo(kzr.x), kz1 = bf_hi(kzr.x), kz2 = bf_lo(kzr.y), kz3 = bf_hi(kzr.y);
    const float ks0 = bf_lo(ksr.x), ks1 = bf_hi(ksr.x), ks2 = bf_lo(ksr.y), ks3 = bf_hi(ksr.y);
    SB();
#pragma unroll
    for (int p = 0; p < 16; ++p) {
      const v4u x = a[p];
      asm volatile("" ::"v"(x));
      if (!FIRST) SCHED(p);
      if (!FIRST) THROTTLE(p);
      else if (p + DK >= ISSUED) asm volatile("s_waitcnt vmcnt(%0)" ::"i"(CAP - 1 + (p + DK - CAP < 16 ? 2 : 0)));
      SB();
      if (p + DK < 16) { if (p + DK >= ISSUED) a[p + DK] = LDK(rk, gb, p + DK); }
      else if (!LAST) a[(p + DK) & 15] = LDK(rk, gbn, (p + DK) & 15);
      else if (p + DK - 16 < DV) a[(p + DK) & 15] = LDV(rv, gbn, (p + DK) & 15);
      if (p == 4) {
        if (LAST) { vsr = ldnt8(rvs, voffT, SOFF_T(jn)); vzr = ldnt8(rvz, voffT, SOFF_T(jn)); }
        else { kzr = ldnt8(rkz, voffS, SOFF_S(jn)); ksr = ldnt8(rks, voffS, SOFF_S(jn)); }
      }
      SB();
      if (p == 0 && !(it & 1)) KSTAMP(it >> 1)
      if (!FIRST && p == 0) frag_read(1);
      if (p == 8) frag_read(0);
      const unsigned d01 = pk_bf16((float)(int)x.x - kz0, (float)(int)x.y - kz1);
      const unsigned d23 = pk_bf16((float)(int)x.z - kz2, (float)(int)x.w - kz3);
      v2u kk2;
      kk2.x = pk_bf16(bf_lo(d01) * ks0, bf_hi(d01) * ks1);
      kk2.y = pk_bf16(bf_lo(d23) * ks2, bf_hi(d23) * ks3);
      *(LAS v2u*)(kbuf + (wr0 ^ (32 * (p & 7))) + 512 * p) = kk2;
      if (!FIRST && p == 1) d1 = qk_mfma();
      if (!FIRST && p == 2) put_logits(it - 1);
      if (p == 9) d0 = qk_mfma();
      SB();
    }
  };
  k_unit(TrueT{}, FalseT{}, j0, 0);
#pragma nounroll
  for (int it = 1; it < NIT - 1; ++it) k_unit(FalseT{}, FalseT{}, (j0 + it * UPI) & (NUNIT - 1), it);
  k_unit(FalseT{}, TrueT{}, (j0 + (NIT - 1) * UPI) & (NUNIT - 1), NIT - 1);
#pragma unroll
  for (int n = DK; n < DV; ++n) a[n] = LDV(rv, UBASE(j0), n);
  SB();
  frag_read(1);
  d1 = qk_mfma();
  put_logits(NIT - 1);

  m = fmaxf(m, dpp<ROR8>(m));
  m = xrow_max(m);
  float lsum = 0.f, csum = 0.f;
  f32x4 acc[2][4];
#pragma unroll
  for (int hf = 0; hf < 2; ++hf)
#pragma unroll
    for (int e = 0; e < 4; ++e) acc[hf][e] = f32x4{0.f, 0.f, 0.f, 0.f};
  float wgt[NWAVE], cterm;
  unsigned sideb;
  const int hsh = 16 * (lane & 1);
  const unsigned tagb = ((tag >> hsh) & 0xffu) | (((tag >> hsh) & 0xff00u) << 8);
  auto v_unit = [&](auto LAST_, int j, int it) __attribute__((always_inline)) {
    constexpr bool LAST = decltype(LAST_)::value;
    const int gb = UBASE(j);
    const int jn = (j + UPI) & (NUNIT - 1);
    const int gbn = UBASE(jn);
    if (LAST) {
#pragma unroll
      for (int n = DV; n < 16; ++n) a[n] = LDV(rv, gb, n);
    }
    const v2u sv = sbuf[it * 64];
    SB();
    v4u pf, vf[4];
    float p0, p1, p2, p3;
#pragma unroll
    for (int n = 0; n < 16; ++n) {
      if (!LAST) {
        asm volatile("" ::"v"(a[n]));
        SCHED(n);
        THROTTLE(n);
        SB();
        a[(n + DV) & 15] = LDV(rv, n + DV < 16 ? gb : gbn, (n + DV) & 15);
        SB();
      }
      if (n == 0 && !(it & 1)) KSTAMP(4 + (it >> 1))
      if (n & 1) {
#pragma unroll
        for (int e = 0; e < 4; ++e) vf[e][(n >> 1) & 3] = pk_bf16((float)(int)a[n - 1][e], (float)(int)a[n][e]);
        asm volatile("" ::"v"(vf[0][(n >> 1) & 3]), "v"(vf[1][(n >> 1) & 3]), "v"(vf[2][(n >> 1) & 3]), "v"(vf[3][(n >> 1) & 3]));
      }
      if (n == 0) {
        p0 = __builtin_amdgcn_exp2f((bf_lo(sv.x) - m) * LOG2E); p1 = __builtin_amdgcn_exp2f((bf_hi(sv.x) - m) * LOG2E);
        p2 = __builtin_amdgcn_exp2f((bf_lo(sv.y) - m) * LOG2E); p3 = __builtin_amdgcn_exp2f((bf_hi(sv.y) - m) * LOG2E);
        lsum += (p0 + p1) + (p2 + p3);
        asm volatile("" ::"v"(lsum));
      }
      if (n == (LAST ? 0 : 2)) {
        const unsigned p01 = pk_bf16(p0 * bf_lo(vsr.x), p1 * bf_hi(vsr.x));
        const unsigned p23 = pk_bf16(p2 * bf_lo(vsr.y), p3 * bf_hi(vsr.y));
        csum = fmaf(bf_lo(p01), bf_lo(vzr.x), csum);
        csum = fmaf(bf_hi(p01), bf_hi(vzr.x), csum);
        csum = fmaf(bf_lo(p23), bf_lo(vzr.y), csum);
        csum = fmaf(bf_hi(p23), bf_hi(vzr.y), csum);
        pf.x = u ? 0u : p01;
        pf.y = u ? 0u : p23;
        pf.z = u ? p01 : 0u;
        pf.w = u ? p23 : 0u;
        asm volatile("" ::"v"(pf), "v"(csum));
      }
      if (!LAST && n == 4) {
        vsr = ldnt8(rvs, voffT, SOFF_T(jn));
        vzr = ldnt8(rvz, voffT, SOFF_T(jn));
      }
      if (LAST && n == 0) {
        const float lw = xrow_sum(lsum + dpp<ROR8>(lsum)), cw = xrow_sum(csum + dpp<ROR8>(csum));
        if (lane < 8) mbuf[w * 8 + h] = f32x4{m, lw, cw, 0.f};
        __syncthreads();
        f32x4 ml[NWAVE];
#pragma unroll
        for (int i = 0; i < NWAVE; ++i) ml[i] = mbuf[i * 8 + 2 * w + hi];
        const float M = fmaxf(fmaxf(ml[0][0], ml[1][0]), fmaxf(ml[2][0], ml[3][0]));
        float L = 0.f, C = 0.f;
#pragma unroll
        for (int i = 0; i < NWAVE; ++i) {
          wgt[i] = __builtin_amdgcn_exp2f((ml[i][0] - M) * LOG2E);
          L = fmaf(wgt[i], ml[i][1], L);
          C = fmaf(wgt[i], ml[i][2], C);
        }
        const float inv = __builtin_amdgcn_rcpf(L);
#pragma unroll
        for (int i = 0; i < NWAVE; ++i) wgt[i] *= inv;
        cterm = -C * inv;
        const unsigned lh = __float_as_uint(fmaf(M, LOG2E, __builtin_amdgcn_logf(L))) >> hsh;
        sideb = tagb | ((lh & 0xffu) << 8) | ((lh & 0xff00u) << 16);
        asm volatile("" ::"v"(wgt[0]), "v"(wgt[1]), "v"(wgt[2]), "v"(wgt[3]), "v"(cterm), "v"(sideb));
      }
      if (EARLYW && LAST && n == 10) {
#pragma unroll
        for (int e = 0; e < 4; ++e) *(LAS f32x4*)((LAS float*)kbuf + c * OSTR + 16 * g + 4 * e) = acc[0][e];
      }
      if ((n & 7) == 7) {
#pragma unroll
        for (int e = 0; e < 4; ++e) acc[n >> 3][e] = mfma16(vf[e], pf, acc[n >> 3][e]);
      }
      SB();
    }
  };
#pragma nounroll
  for (int it = 0; it < NIT - 1; ++it) v_unit(FalseT{}, (j0 + it * UPI) & (NUNIT - 1), it);
  v_unit(TrueT{}, (j0 + (NIT - 1) * UPI) & (NUNIT - 1), NIT - 1);
  {
    LAS float* obuf = (LAS float*)kbuf + c * OSTR + 16 * g;
#pragma unroll
    for (int hf = EARLYW; hf < 2; ++hf)
#pragma unroll
      for (int e = 0; e < 4; ++e) *(LAS f32x4*)(obuf + 64 * hf + 4 * e) = acc[hf][e];
  }
  __syncthreads();
  {
    f32x4 ob[NWAVE][2];
#pragma unroll
    for (int i = 0; i < NWAVE; ++i) {
      const LAS float* op = (const LAS float*)((LAS unsigned char*)smem + i * WBYTES) + (2 * w + hi) * OSTR + 4 * (lane & 31);
      ob[i][0] = *(const LAS f32x4*)op;
      ob[i][1] = *(const LAS f32x4*)(op + 8 * OSTR);
    }
    f32x4 o = {cterm, cterm, cterm, cterm};
#pragma unroll
    for (int i = 0; i < NWAVE; ++i) o += wgt[i] * (ob[i][0] + ob[i][1]);
    const v4u pk = {(__float_as_uint(o[0]) & 0xffffff00u) | (sideb & 0xffu), (__float_as_uint(o[1]) & 0xffffff00u) | ((sideb >> 8) & 0xffu),
                    (__float_as_uint(o[2]) & 0xffffff00u) | ((sideb >> 16) & 0xffu), (__float_as_uint(o[3]) & 0xffffff00u) | (sideb >> 24)};
    const int cons = 16 * w + (lane >> 2);
    __builtin_amdgcn_raw_buffer_store_b128(pk, rp, (cons * NSPLIT + ((cb + SWZ * cons) & (NSPLIT - 1))) * 64 + (lane & 3) * 16, 0, 16);
  }
  asm volatile("s_waitcnt vmcnt(0)" ::: "memory");
  __builtin_amdgcn_s_sleep(FIRST_SLEEP);
  {
    const int pr = 16 * w + (lane >> 2);
    constexpr int OOB = 0x40000000;
    int po = (cb * NSPLIT + pr) * 64 + (lane & 3) * 16;
    v4u xo = {0u, 0u, 0u, 0u};
    unsigned spins = 0;
    bool bad = false;
    while (true) {
      asm volatile("" ::: "memory");
      const v4u x = __builtin_amdgcn_raw_buffer_load_b128(rp, po, 0, 16);
      unsigned long long okm = __ballot(((x.x & 0xffu) | ((x.z & 0xffu) << 16)) == tagb);
      okm &= okm >> 1;
      okm &= okm >> 2;
      okm = (okm & 0x1111111111111111ull) * 15ull;
      const bool k = (okm >> lane) & 1ull;
      xo = k ? x : xo;
      po = k ? OOB : po;
      const int miss = __builtin_popcountll(__ballot(po != OOB));
      if (miss == 0) break;
      if (++spins > SPIN_CAP) { bad = true; break; }
      if (miss > 4 * FETCH_MISS) __builtin_amdgcn_s_sleep(SLOW_SLEEP);
      else if (POLL_SLEEP) __builtin_amdgcn_s_sleep(POLL_SLEEP);
    }
    const unsigned lhalf = ((xo.y & 0xffu) | ((xo.w & 0xffu) << 8)) << hsh;
    float lse = __uint_as_float(lhalf | (unsigned)__builtin_amdgcn_mov_dpp((int)lhalf, 0xB1, 0xf, 0xf, true));
    if (bad) lse = __builtin_nanf("");
    float M = fmaxf(lse, dpp<ROR4>(lse));
    M = fmaxf(M, dpp<ROR8>(M));
    M = xrow_max(M);
    const float e = __builtin_amdgcn_exp2f(lse - M);
    float t;
    {
      float av[4];
#pragma unroll
      for (int i = 0; i < 4; ++i) av[i] = e * __uint_as_float(xo[i] & 0xffffff00u);
      auto s02 = __builtin_amdgcn_permlane32_swap(__float_as_uint(av[0]), __float_as_uint(av[2]), false, false);
      auto s13 = __builtin_amdgcn_permlane32_swap(__float_as_uint(av[1]), __float_as_uint(av[3]), false, false);
      const float b0 = __uint_as_float(s02[0]) + __uint_as_float(s02[1]);
      const float b1 = __uint_as_float(s13[0]) + __uint_as_float(s13[1]);
      auto sw = __builtin_amdgcn_permlane16_swap(__float_as_uint(b0), __float_as_uint(b1), false, false);
      t = __uint_as_float(sw[0]) + __uint_as_float(sw[1]);
    }
    t += dpp<ROR4>(t);
    t += dpp<ROR8>(t);
    float L = e + dpp<ROR4>(e);
    L = xrow_sum(L + dpp<ROR8>(L));
    LAS f32x4* fin = (LAS f32x4*)((LAS unsigned char*)smem + NWAVE * WBYTES + NWAVE * 8 * 16);
    if (c < 4) fin[w * 16 + 4 * g + c] = f32x4{t, M, L, 0.f};
    __syncthreads();
    if (tid < 16) {
      f32x4 r[NWAVE];
#pragma unroll
      for (int i = 0; i < NWAVE; ++i) r[i] = fin[i * 16 + tid];
      const float Mx = fmaxf(fmaxf(r[0][1], r[1][1]), fmaxf(r[2][1], r[3][1]));
      float Lx = 0.f, ox = 0.f;
#pragma unroll
      for (int i = 0; i < NWAVE; ++i) {
        const float ee = __builtin_amdgcn_exp2f(r[i][1] - Mx);
        Lx = fmaf(ee, r[i][2], Lx);
        ox = fmaf(ee, r[i][0], ox);
      }
      out[(hk * 8 + (cb >> 3)) * DH + 16 * (cb & 7) + tid] = (unsigned short)pk_bf16(ox * __builtin_amdgcn_rcpf(Lx), 0.f);
      if (cb == 0 && tid == 0) __hip_atomic_store((gu32*)ws + hk * 64, tag, RLX_AGENT);
    }
  }
}
}

extern "C" void kernel_launch(void* const* d_in, const int* in_sizes, int n_in, void* d_out, int out_size, void* d_ws,
                              size_t ws_size, hipStream_t stream) {
  kv_decode<<<HKV * NSPLIT, NTHR, 0, stream>>>((const unsigned short*)d_in[0], (const int*)d_in[1], (const int*)d_in[2],
                                               (const unsigned short*)d_in[3], (const unsigned short*)d_in[4],
                                               (const unsigned short*)d_in[5], (const unsigned short*)d_in[6],
                                               (unsigned char*)d_ws, (unsigned short*)d_out);
}
```

```cpp
#define XMAP 2
#define T_UNIT1 350
#include <hip/hip_runtime.h>
#include <hip/hip_bf16.h>
#include <stdint.h>

#define DK 6
#define DV 6
#define BURST 16
#define POLL_SLEEP 2
#define FETCH_MISS 4
#define SWZ 1
#define FIRST_SLEEP 8
#define ONEW 0
#define EARLYW 1
#define KFIRST 4
#define VTOUCH 0
#define CODE_KB 16
#define PLAY 1
#define CALIB 0
#define MARGIN 30
#define TAU2 219
#define SLOW_SLEEP 4
#define CAP 5
#define CAP2 0
#define LATE2 50
#define CHK 4
#define TAU 218
#define HSTAG 2
#define LDS_PAD 0

namespace {
constexpr int HKV = 4, SKV = 65536, DH = 128, GS = 128, NGRP = SKV / GS;
constexpr int NWAVE = 4, NTHR = NWAVE * 64;
constexpr int NSPLIT = 64;
constexpr int NUNIT = NGRP * 4;
constexpr int UPI = NSPLIT * NWAVE;
constexpr int NIT = NUNIT / UPI;
constexpr int OSTR = 132;
constexpr int WBYTES = 16 * OSTR * 4;
constexpr size_t WS_PAY = 4096;
constexpr size_t WS_PROG = WS_PAY + (size_t)HKV * NSPLIT * NSPLIT * 64;
constexpr unsigned SPIN_CAP = 1u << 20;

typedef unsigned v4u __attribute__((ext_vector_type(4)));
typedef unsigned v2u __attribute__((ext_vector_type(2)));
typedef float f32x4 __attribute__((ext_vector_type(4)));
typedef float f32x2 __attribute__((ext_vector_type(2)));
typedef __bf16 bf16x8 __attribute__((ext_vector_type(8)));
typedef __bf16 bf16x2 __attribute__((ext_vector_type(2)));
#define LAS __attribute__((address_space(3)))
#define GAS __attribute__((address_space(1)))
typedef GAS unsigned gu32;
typedef GAS unsigned long long gu64;
#define RLX_AGENT __ATOMIC_RELAXED, __HIP_MEMORY_SCOPE_AGENT
#define RSRC(p, n) __builtin_amdgcn_make_buffer_rsrc((void*)(p), 0, (n), 0x00020000)

__device__ __forceinline__ unsigned pk_bf16(float lo, float hi) {
  f32x2 v = {lo, hi};
  return __builtin_bit_cast(unsigned, __builtin_convertvector(v, bf16x2));
}
__device__ __forceinline__ float bf_lo(unsigned p) { return __uint_as_float(p << 16); }
__device__ __forceinline__ float bf_hi(unsigned p) { return __uint_as_float(p & 0xffff0000u); }
__device__ __forceinline__ f32x4 mfma16(v4u a, v4u b, f32x4 c) {
  return __builtin_amdgcn_mfma_f32_16x16x32_bf16(__builtin_bit_cast(bf16x8, a), __builtin_bit_cast(bf16x8, b), c, 0, 0, 0);
}
__device__ __forceinline__ v4u ldnt(__amdgpu_buffer_rsrc_t rs, int voff, int soff) {
  return __builtin_amdgcn_raw_buffer_load_b128(rs, voff, soff, 2);
}
__device__ __forceinline__ v2u ldnt8(__amdgpu_buffer_rsrc_t rs, int voff, int soff) {
  return __builtin_amdgcn_raw_buffer_load_b64(rs, voff, soff, 2);
}
template <int CTRL> __device__ __forceinline__ float dpp(float x) {
  return __int_as_float(__builtin_amdgcn_mov_dpp(__float_as_int(x), CTRL, 0xf, 0xf, true));
}
constexpr int ROR1 = 0x121, ROR2 = 0x122, ROR4 = 0x124, ROR8 = 0x128;
__device__ __forceinline__ float xrow_max(float x) {
  auto s = __builtin_amdgcn_permlane16_swap(__float_as_uint(x), __float_as_uint(x), false, false);
  x = fmaxf(__uint_as_float(s[0]), __uint_as_float(s[1]));
  auto t = __builtin_amdgcn_permlane32_swap(__float_as_uint(x), __float_as_uint(x), false, false);
  return fmaxf(__uint_as_float(t[0]), __uint_as_float(t[1]));
}
__device__ __forceinline__ float xrow_sum(float x) {
  auto s = __builtin_amdgcn_permlane16_swap(__float_as_uint(x), __float_as_uint(x), false, false);
  x = __uint_as_float(s[0]) + __uint_as_float(s[1]);
  auto t = __builtin_amdgcn_permlane32_swap(__float_as_uint(x), __float_as_uint(x), false, false);
  return __uint_as_float(t[0]) + __uint_as_float(t[1]);
}
__device__ __forceinline__ float row_sum(float x) {
  x += dpp<ROR1>(x);
  x += dpp<ROR2>(x);
  x += dpp<ROR4>(x);
  return x + dpp<ROR8>(x);
}

template <int N> struct IntT { static constexpr int value = N; };
struct TrueT { static constexpr bool value = true; };
struct FalseT { static constexpr bool value = false; };
constexpr float LOG2E = 1.4426950408889634f;
constexpr float SCALE = 0.08838834764831845f;

__global__ void __launch_bounds__(NTHR, 2)
kv_decode(const unsigned short* __restrict__ q, const int* __restrict__ qk, const int* __restrict__ qv,
          const unsigned short* __restrict__ ks, const unsigned short* __restrict__ kz,
          const unsigned short* __restrict__ vs, const unsigned short* __restrict__ vz,
          unsigned char* ws, unsigned short* __restrict__ out) {
  __shared__ __attribute__((aligned(16))) unsigned char smem[NWAVE * WBYTES + NWAVE * 8 * 16 + NWAVE * NIT * 64 * 8 + LDS_PAD];
  const int tid = threadIdx.x, lane = tid & 63;
  unsigned long long pc0;
  asm volatile("s_getpc_b64 %0" : "=s"(pc0));
  const unsigned cw = __builtin_amdgcn_raw_buffer_load_b32(RSRC(pc0 & ~255ull, CODE_KB * 1024), ((tid & 31) + 32 * ((blockIdx.x >> 3) & 7)) * 128 | (tid & 224) << 22, 0, 0);
  __builtin_amdgcn_sched_barrier(0);
  asm volatile("" ::"s"(qk), "s"(kz), "s"(out));
  const int w = __builtin_amdgcn_readfirstlane(tid >> 6);
  const int hk = blockIdx.x & 3, cb = (blockIdx.x >> 3) + 32 * ((blockIdx.x >> 2) & 1);
  const int rot = ((blockIdx.x >> 3) + 2 * w) & 7;
  constexpr int HEADB = SKV * DH * 4;
  const char* kh = (const char*)qk + (size_t)hk * HEADB;
  const __amdgpu_buffer_rsrc_t rk = RSRC(kh, HEADB);
  const int voffK = lane * 16;
#define UBASE(j) (((j) >> 1) * 32768 + ((j) & 1) * 2048)
#define LDK(rs, gb, p) ldnt(rs, voffK + ((p) & 1) * 1024, (gb) + ((((p) >> 1) ^ rot) << 12))
#define LDV(rs, gb, n) ldnt(rs, (((n) & 4) ? voffV1 : voffV0) + ((n) & 3) * 512 + ((n) >> 3) * 256, (gb))
#define SOFF_S(j) (((j) >> 2) * 256)
#define SOFF_T(j) (((j) >> 1) * 128 + ((j) & 1) * 8)
  const int j0 = (cb * NWAVE + w + HSTAG * hk * UPI) & (NUNIT - 1);
  v4u a[16];
#pragma unroll
  for (int i = 0; i < KFIRST; ++i) a[i] = LDK(rk, UBASE(j0), i);
  __builtin_amdgcn_sched_barrier(0);
  unsigned due = (unsigned)__builtin_amdgcn_s_memrealtime() + T_UNIT1;
  const __amdgpu_buffer_rsrc_t rks = RSRC(ks + (size_t)hk * NGRP * DH, NGRP * DH * 2);
  const __amdgpu_buffer_rsrc_t rkz = RSRC(kz + (size_t)hk * NGRP * DH, NGRP * DH * 2);
  const int voffS = 8 * (lane & 31);
  v2u kzr = ldnt8(rkz, voffS, SOFF_S(j0));
  v2u ksr = ldnt8(rks, voffS, SOFF_S(j0));
#pragma unroll
  for (int i = KFIRST; i < 8; ++i) a[i] = LDK(rk, UBASE(j0), i);
  __builtin_amdgcn_sched_barrier(0);
  const int g = lane >> 4, c = lane & 15, u = (lane >> 3) & 1, h = lane & 7;
  v4u qf[4];
#pragma unroll
  for (int kk = 0; kk < 4; ++kk) qf[kk] = __builtin_nontemporal_load((const v4u*)(q + (size_t)(hk * 8 + h) * DH + 32 * kk + 8 * g));
  __builtin_amdgcn_sched_barrier(0);
#pragma unroll
  for (int i = 8; i < BURST; ++i) a[i] = LDK(rk, UBASE(j0), i);
  __builtin_amdgcn_sched_barrier(0);
  const __amdgpu_buffer_rsrc_t rp = RSRC(ws + WS_PAY + (size_t)hk * NSPLIT * NSPLIT * 64, NSPLIT * NSPLIT * 64);
  unsigned tag = *(const volatile __attribute__((address_space(4))) unsigned*)(ws + hk * 256) + 0x01010101u;
  tag = tag ? tag : 0x01010101u;
  __builtin_amdgcn_sched_barrier(0);

  const __amdgpu_buffer_rsrc_t rv = RSRC((const char*)qv + (size_t)hk * HEADB, HEADB);
  const __amdgpu_buffer_rsrc_t rvs = RSRC(vs + (size_t)hk * SKV, SKV * 2);
  const __amdgpu_buffer_rsrc_t rvz = RSRC(vz + (size_t)hk * SKV, SKV * 2);
  const int voffV0 = ((g ^ rot) << 12) + c * 16, voffV1 = (((4 + g) ^ rot) << 12) + c * 16;
  const int voffT = 16 * ((4 * u + g) ^ rot);
  LAS unsigned char* kbuf = (LAS unsigned char*)smem + w * WBYTES;
  LAS f32x4* mbuf = (LAS f32x4*)((LAS unsigned char*)smem + NWAVE * WBYTES);
  LAS v2u* sbuf = (LAS v2u*)((LAS unsigned char*)smem + NWAVE * WBYTES + NWAVE * 8 * 16) + w * (NIT * 64) + lane;
  const int hi = lane >> 5;
  const int wr0 = hi * 256 + ((((lane & 31) >> 1) ^ hi) << 4) + 8 * (lane & 1);
  float m = -INFINITY;
  v2u vsr, vzr;
#define KSTAMP(it_) {}

#define SB() __builtin_amdgcn_sched_barrier(0)
  int ahead = 1;
#define WAITN(c, step) ((c) - 1 + ((((step) & 15) > 4 && ((step) & 15) <= 4 + (c)) ? ((step) >> 4) : 0))
  unsigned now = 0;
  int tau = TAU;
  v2u pg;
  const __amdgpu_buffer_rsrc_t rg = RSRC(ws + WS_PROG + (size_t)(blockIdx.x >> 6) * 16 * 2048, 16 * 2048);
  const int pgw = tid < NWAVE ? tid * 512 + (blockIdx.x & 63) * 8 : 0x40000000, pgr = tid * 8;
#define POST(U) do { \
    __builtin_amdgcn_raw_buffer_store_b64(v2u{now, ~now}, rg, pgw | ((~CALIB >> (U)) & 1) << 30, (U) * 2048, 16); \
    pg = __builtin_amdgcn_raw_buffer_load_b64(rg, pgr | ((~(CALIB * 4) >> (U)) & 1) << 30, (((U) - 2) & 15) * 2048, 16); } while (0)
#define ANCHOR(U) do { if ((CALIB * 4 >> (U)) & 1) { \
    const bool ok_ = (pg.y == ~pg.x) & (now - pg.x < 1500u); \
    const int cnt_ = __builtin_popcountll(__ballot(ok_)); \
    const float sum_ = row_sum(xrow_sum(ok_ ? (float)(int)(pg.x - now) : 0.f)); \
    const int rel_ = __builtin_amdgcn_readfirstlane((int)(sum_ * __builtin_amdgcn_rcpf((float)cnt_))); \
    if (cnt_ > 40) { tau = TAU2; due = now + rel_ + (2 * TAU2 - TAU2 / 4 + MARGIN) + ((U) == 8 ? 20 : 0); } } } while (0)
#define THROTTLE(step) asm volatile("s_cmp_lt_i32 %0, 0\n\ts_cbranch_scc1 1f\n\ts_waitcnt vmcnt(%1)\n1:" ::"s"(ahead), "i"(WAITN(CAP, step)) : "scc")
#define SCHED(step) do { if ((step) % CHK == 0) { now = (unsigned)__builtin_amdgcn_s_memrealtime(); ahead = (int)(due + (step) * TAU / 16 - now); if ((step) + CHK >= 16) due += tau; } } while (0)
  f32x4 d0, d1;
  v4u kf[4];
  auto frag_read = [&](int half) __attribute__((always_inline)) {
#pragma unroll
    for (int kk = 0; kk < 4; ++kk) kf[kk] = *(LAS v4u*)(kbuf + c * 256 + (((4 * kk + g) ^ c) << 4) + 4096 * half);
  };
  auto qk_mfma = [&]() __attribute__((always_inline)) {
    f32x4 d = {0.f, 0.f, 0.f, 0.f};
#pragma unroll
    for (int kk = 0; kk < 4; ++kk) d = mfma16(kf[kk], qf[kk], d);
    return d;
  };
  auto put_logits = [&](int it) __attribute__((always_inline)) {
    const unsigned r01 = pk_bf16(u ? d1[0] : d0[0], u ? d1[1] : d0[1]);
    const unsigned r23 = pk_bf16(u ? d1[2] : d0[2], u ? d1[3] : d0[3]);
    v2u sv;
    sv.x = pk_bf16(bf_lo(r01) * SCALE, bf_hi(r01) * SCALE);
    sv.y = pk_bf16(bf_lo(r23) * SCALE, bf_hi(r23) * SCALE);
    sbuf[it * 64] = sv;
    m = fmaxf(fmaxf(m, fmaxf(bf_lo(sv.x), bf_hi(sv.x))), fmaxf(bf_lo(sv.y), bf_hi(sv.y)));
    asm volatile("" ::"v"(m));
  };
  auto k_unit = [&](auto FIRST_, auto LAST_, int j, int it) __attribute__((always_inline)) {
    constexpr bool FIRST = decltype(FIRST_)::value, LAST = decltype(LAST_)::value;
    constexpr int ISSUED = FIRST ? BURST : DK;
    const int gb = UBASE(j);
    const int jn = LAST ? j0 : (j + UPI) & (NUNIT - 1);
    const int gbn = UBASE(jn);
    const float kz0 = bf_lo(kzr.x), kz1 = bf_hi(kzr.x), kz2 = bf_lo(kzr.y), kz3 = bf_hi(kzr.y);
    const float ks0 = bf_lo(ksr.x), ks1 = bf_hi(ksr.x), ks2 = bf_lo(ksr.y), ks3 = bf_hi(ksr.y);
    SB();
#pragma unroll
    for (int p = 0; p < 16; ++p) {
      const v4u x = a[p];
      asm volatile("" ::"v"(x));
      if (!FIRST) SCHED(p);
      if (!FIRST) THROTTLE(p + 16 * ((LAST || !CALIB) ? 2 : 4));
      else if (p + DK >= ISSUED) asm volatile("s_waitcnt vmcnt(%0)" ::"i"(CAP - 1 + (p + DK - CAP < 16 ? 2 + 3 * VTOUCH : 0)));
      SB();
      if (p + DK < 16) { if (p + DK >= ISSUED) a[p + DK] = LDK(rk, gb, p + DK); }
      else if (!LAST) a[(p + DK) & 15] = LDK(rk, gbn, (p + DK) & 15);
      else if (p + DK - 16 < DV) a[(p + DK) & 15] = LDV(rv, gbn, (p + DK) & 15);
      if (p == 4) {
        if (LAST && !VTOUCH) { vsr = ldnt8(rvs, voffT, SOFF_T(jn)); vzr = ldnt8(rvz, voffT, SOFF_T(jn)); }
        else if (!LAST) { kzr = ldnt8(rkz, voffS, SOFF_S(jn)); ksr = ldnt8(rks, voffS, SOFF_S(jn)); }
        if (CALIB && !FIRST && !LAST) POST(it);
      }
      SB();
      if (CALIB && !FIRST && !LAST && p == 11) ANCHOR(it);
      if (p == 0 && !(it & 1)) KSTAMP(it >> 1)
      if (!FIRST && p == 0) frag_read(1);
      if (p == 8) frag_read(0);
      const unsigned d01 = pk_bf16((float)(int)x.x - kz0, (float)(int)x.y - kz1);
      const unsigned d23 = pk_bf16((float)(int)x.z - kz2, (float)(int)x.w - kz3);
      v2u kk2;
      kk2.x = pk_bf16(bf_lo(d01) * ks0, bf_hi(d01) * ks1);
      kk2.y = pk_bf16(bf_lo(d23) * ks2, bf_hi(d23) * ks3);
      *(LAS v2u*)(kbuf + (wr0 ^ (32 * (p & 7))) + 512 * p) = kk2;
      if (!FIRST && p == 1) d1 = qk_mfma();
      if (!FIRST && p == 2) put_logits(it - 1);
      if (p == 9) d0 = qk_mfma();
      SB();
    }
  };
  asm volatile("" ::"v"(cw));
  k_unit(TrueT{}, FalseT{}, j0, 0);
#pragma nounroll
  for (int it = 1; it < NIT - 1; ++it) k_unit(FalseT{}, FalseT{}, (j0 + it * UPI) & (NUNIT - 1), it);
  k_unit(FalseT{}, TrueT{}, (j0 + (NIT - 1) * UPI) & (NUNIT - 1), NIT - 1);
#pragma unroll
  for (int n = DK; n < DV; ++n) a[n] = LDV(rv, UBASE(j0), n);
  SB();
  frag_read(1);
  d1 = qk_mfma();
  put_logits(NIT - 1);

  m = fmaxf(m, dpp<ROR8>(m));
  m = xrow_max(m);
  float lsum = 0.f, csum = 0.f;
  f32x4 acc[2][4];
#pragma unroll
  for (int hf = 0; hf < 2; ++hf)
#pragma unroll
    for (int e = 0; e < 4; ++e) acc[hf][e] = f32x4{0.f, 0.f, 0.f, 0.f};
  float wgt[NWAVE], cterm;
  unsigned sideb;
  const int hsh = 16 * (lane & 1);
  const unsigned tagb = ((tag >> hsh) & 0xffu) | (((tag >> hsh) & 0xff00u) << 8);
  auto v_unit = [&](auto LAST_, int j, int it) __attribute__((always_inline)) {
    constexpr bool LAST = decltype(LAST_)::value;
    const int gb = UBASE(j);
    const int jn = (j + UPI) & (NUNIT - 1);
    const int gbn = UBASE(jn);
    if (LAST) {
#pragma unroll
      for (int n = DV; n < 16; ++n) a[n] = LDV(rv, gb, n);
    }
    const v2u sv = sbuf[it * 64];
    SB();
    v4u pf, vf[4];
    float p0, p1, p2, p3;
#pragma unroll
    for (int n = 0; n < 16; ++n) {
      if (!LAST) {
        asm volatile("" ::"v"(a[n]));
        SCHED(n);
        THROTTLE(n + 16 * (CALIB ? 4 : 2));
        SB();
        a[(n + DV) & 15] = LDV(rv, n + DV < 16 ? gb : gbn, (n + DV) & 15);
        SB();
      }
      if (n == 0 && !(it & 1)) KSTAMP(4 + (it >> 1))
      if (n & 1) {
#pragma unroll
        for (int e = 0; e < 4; ++e) vf[e][(n >> 1) & 3] = pk_bf16((float)(int)a[n - 1][e], (float)(int)a[n][e]);
        asm volatile("" ::"v"(vf[0][(n >> 1) & 3]), "v"(vf[1][(n >> 1) & 3]), "v"(vf[2][(n >> 1) & 3]), "v"(vf[3][(n >> 1) & 3]));
      }
      if (n == 0) {
        p0 = __builtin_amdgcn_exp2f((bf_lo(sv.x) - m) * LOG2E); p1 = __builtin_amdgcn_exp2f((bf_hi(sv.x) - m) * LOG2E);
        p2 = __builtin_amdgcn_exp2f((bf_lo(sv.y) - m) * LOG2E); p3 = __builtin_amdgcn_exp2f((bf_hi(sv.y) - m) * LOG2E);
        lsum += (p0 + p1) + (p2 + p3);
        asm volatile("" ::"v"(lsum));
      }
      if (n == (LAST ? 0 : 2)) {
        const unsigned p01 = pk_bf16(p0 * bf_lo(vsr.x), p1 * bf_hi(vsr.x));
        const unsigned p23 = pk_bf16(p2 * bf_lo(vsr.y), p3 * bf_hi(vsr.y));
        csum = fmaf(bf_lo(p01), bf_lo(vzr.x), csum);
        csum = fmaf(bf_hi(p01), bf_hi(vzr.x), csum);
        csum = fmaf(bf_lo(p23), bf_lo(vzr.y), csum);
        csum = fmaf(bf_hi(p23), bf_hi(vzr.y), csum);
        pf.x = u ? 0u : p01;
        pf.y = u ? 0u : p23;
        pf.z = u ? p01 : 0u;
        pf.w = u ? p23 : 0u;
        asm volatile("" ::"v"(pf), "v"(csum));
      }
      if (!LAST && n == 4) {
        vsr = ldnt8(rvs, voffT, SOFF_T(jn));
        vzr = ldnt8(rvz, voffT, SOFF_T(jn));
        if (CALIB) POST(NIT + it);
      }
      if (CALIB && !LAST && n == 10) ANCHOR(NIT + it);
      if (LAST && n == 0) {
        const float lw = xrow_sum(lsum + dpp<ROR8>(lsum)), cw = xrow_sum(csum + dpp<ROR8>(csum));
        if (lane < 8) mbuf[w * 8 + h] = f32x4{m, lw, cw, 0.f};
        __syncthreads();
        f32x4 ml[NWAVE];
#pragma unroll
        for (int i = 0; i < NWAVE; ++i) ml[i] = mbuf[i * 8 + 2 * w + hi];
        const float M = fmaxf(fmaxf(ml[0][0], ml[1][0]), fmaxf(ml[2][0], ml[3][0]));
        float L = 0.f, C = 0.f;
#pragma unroll
        for (int i = 0; i < NWAVE; ++i) {
          wgt[i] = __builtin_amdgcn_exp2f((ml[i][0] - M) * LOG2E);
          L = fmaf(wgt[i], ml[i][1], L);
          C = fmaf(wgt[i], ml[i][2], C);
        }
        const float inv = __builtin_amdgcn_rcpf(L);
#pragma unroll
        for (int i = 0; i < NWAVE; ++i) wgt[i] *= inv;
        cterm = -C * inv;
        const unsigned lh = __float_as_uint(fmaf(M, LOG2E, __builtin_amdgcn_logf(L))) >> hsh;
        sideb = tagb | ((lh & 0xffu) << 8) | ((lh & 0xff00u) << 16);
        asm volatile("" ::"v"(wgt[0]), "v"(wgt[1]), "v"(wgt[2]), "v"(wgt[3]), "v"(cterm), "v"(sideb));
      }
      if (EARLYW && LAST && n == 10) {
#pragma unroll
        for (int e = 0; e < 4; ++e) *(LAS f32x4*)((LAS float*)kbuf + c * OSTR + 16 * g + 4 * e) = acc[0][e];
      }
      if ((n & 7) == 7) {
#pragma unroll
        for (int e = 0; e < 4; ++e) acc[n >> 3][e] = mfma16(vf[e], pf, acc[n >> 3][e]);
      }
      SB();
    }
  };
#pragma nounroll
  for (int it = 0; it < NIT - 1; ++it) v_unit(FalseT{}, (j0 + it * UPI) & (NUNIT - 1), it);
  v_unit(TrueT{}, (j0 + (NIT - 1) * UPI) & (NUNIT - 1), NIT - 1);
  {
    LAS float* obuf = (LAS float*)kbuf + c * OSTR + 16 * g;
#pragma unroll
    for (int hf = EARLYW; hf < 2; ++hf)
#pragma unroll
      for (int e = 0; e < 4; ++e) *(LAS f32x4*)(obuf + 64 * hf + 4 * e) = acc[hf][e];
  }
  __syncthreads();
  {
    f32x4 ob[NWAVE][2];
#pragma unroll
    for (int i = 0; i < NWAVE; ++i) {
      const LAS float* op = (const LAS float*)((LAS unsigned char*)smem + i * WBYTES) + (2 * w + hi) * OSTR + 4 * (lane & 31);
      ob[i][0] = *(const LAS f32x4*)op;
      ob[i][1] = *(const LAS f32x4*)(op + 8 * OSTR);
    }
    f32x4 o = {cterm, cterm, cterm, cterm};
#pragma unroll
    for (int i = 0; i < NWAVE; ++i) o += wgt[i] * (ob[i][0] + ob[i][1]);
    const v4u pk = {(__float_as_uint(o[0]) & 0xffffff00u) | (sideb & 0xffu), (__float_as_uint(o[1]) & 0xffffff00u) | ((sideb >> 8) & 0xffu),
                    (__float_as_uint(o[2]) & 0xffffff00u) | ((sideb >> 16) & 0xffu), (__float_as_uint(o[3]) & 0xffffff00u) | (sideb >> 24)};
    const int cons = 16 * w + (lane >> 2);
    __builtin_amdgcn_raw_buffer_store_b128(pk, rp, ((cons >> 1) * NSPLIT + ((cb + SWZ * (cons >> 1)) & (NSPLIT - 1))) * 128 + (lane & 7) * 16, 0, 16);
  }
  asm volatile("s_waitcnt vmcnt(0)" ::: "memory");
  __builtin_amdgcn_s_sleep(FIRST_SLEEP);
  {
    const int pr = 16 * w + (lane >> 2);
    constexpr int OOB = 0x40000000;
    int po = ((cb >> 1) * NSPLIT + pr) * 128 + (cb & 1) * 64 + (lane & 3) * 16;
    v4u xo = {0u, 0u, 0u, 0u};
    unsigned spins = 0;
    bool bad = false;
    while (true) {
      asm volatile("" ::: "memory");
      const v4u x = __builtin_amdgcn_raw_buffer_load_b128(rp, po, 0, 16);
      unsigned long long okm = __ballot(((x.x & 0xffu) | ((x.z & 0xffu) << 16)) == tagb);
      okm &= okm >> 1;
      okm &= okm >> 2;
      okm = (okm & 0x1111111111111111ull) * 15ull;
      const bool k = (okm >> lane) & 1ull;
      xo = k ? x : xo;
      po = k ? OOB : po;
      const int miss = __builtin_popcountll(__ballot(po != OOB));
      if (miss == 0) break;
      if (++spins > SPIN_CAP) { bad = true; break; }
      if (miss > 4 * FETCH_MISS) __builtin_amdgcn_s_sleep(SLOW_SLEEP);
      else if (POLL_SLEEP) __builtin_amdgcn_s_sleep(POLL_SLEEP);
    }
    const unsigned lhalf = ((xo.y & 0xffu) | ((xo.w & 0xffu) << 8)) << hsh;
    float lse = __uint_as_float(lhalf | (unsigned)__builtin_amdgcn_mov_dpp((int)lhalf, 0xB1, 0xf, 0xf, true));
    if (bad) lse = __builtin_nanf("");
    float M = fmaxf(lse, dpp<ROR4>(lse));
    M = fmaxf(M, dpp<ROR8>(M));
    M = xrow_max(M);
    const float e = __builtin_amdgcn_exp2f(lse - M);
    float t;
    {
      float av[4];
#pragma unroll
      for (int i = 0; i < 4; ++i) av[i] = e * __uint_as_float(xo[i] & 0xffffff00u);
      auto s02 = __builtin_amdgcn_permlane32_swap(__float_as_uint(av[0]), __float_as_uint(av[2]), false, false);
      auto s13 = __builtin_amdgcn_permlane32_swap(__float_as_uint(av[1]), __float_as_uint(av[3]), false, false);
      const float b0 = __uint_as_float(s02[0]) + __uint_as_float(s02[1]);
      const float b1 = __uint_as_float(s13[0]) + __uint_as_float(s13[1]);
      auto sw = __builtin_amdgcn_permlane16_swap(__float_as_uint(b0), __float_as_uint(b1), false, false);
      t = __uint_as_float(sw[0]) + __uint_as_float(sw[1]);
    }
    t += dpp<ROR4>(t);
    t += dpp<ROR8>(t);
    float L = e + dpp<ROR4>(e);
    L = xrow_sum(L + dpp<ROR8>(L));
    LAS f32x4* fin = (LAS f32x4*)((LAS unsigned char*)smem + NWAVE * WBYTES + NWAVE * 8 * 16);
    if (c < 4) fin[w * 16 + 4 * g + c] = f32x4{t, M, L, 0.f};
    __syncthreads();
    if (tid < 16) {
      f32x4 r[NWAVE];
#pragma unroll
      for (int i = 0; i < NWAVE; ++i) r[i] = fin[i * 16 + tid];
      const float Mx = fmaxf(fmaxf(r[0][1], r[1][1]), fmaxf(r[2][1], r[3][1]));
      float Lx = 0.f, ox = 0.f;
#pragma unroll
      for (int i = 0; i < NWAVE; ++i) {
        const float ee = __builtin_amdgcn_exp2f(r[i][1] - Mx);
        Lx = fmaf(ee, r[i][2], Lx);
        ox = fmaf(ee, r[i][0], ox);
      }
      out[(hk * 8 + (cb >> 3)) * DH + 16 * (cb & 7) + tid] = (unsigned short)pk_bf16(ox * __builtin_amdgcn_rcpf(Lx), 0.f);
      if (cb == 0 && tid == 0) __hip_atomic_store((gu32*)ws + hk * 64, tag, RLX_AGENT);
    }
  }
}
}

extern "C" void kernel_launch(void* const* d_in, const int* in_sizes, int n_in, void* d_out, int out_size, void* d_ws,
                              size_t ws_size, hipStream_t stream) {
  kv_decode<<<HKV * NSPLIT, NTHR, 0, stream>>>((const unsigned short*)d_in[0], (const int*)d_in[1], (const int*)d_in[2],
                                               (const unsigned short*)d_in[3], (const unsigned short*)d_in[4],
                                               (const unsigned short*)d_in[5], (const unsigned short*)d_in[6],
                                               (unsigned char*)d_ws, (unsigned short*)d_out);
}
```

```cpp
#define CODE_KB 18
#define CAP2 6
#define DIST 8
#define T_UNIT1 370
#define SLACK0 35
#define SLACK1 4
#include <hip/hip_runtime.h>
#include <hip/hip_bf16.h>
#include <stdint.h>

#define CAP 5
#define TAU 218
#define FIRST_SLEEP 8
#define DK DIST
#define DV DIST
#define BURST 16
#define KFIRST 4
#define CHK 4
#define HSTAG 2
#define POLL_SLEEP 2
#define SLOW_SLEEP 4
#define FETCH_MISS 4
#define LDS_PAD 0

namespace {
constexpr int HKV = 4, SKV = 65536, DH = 128, GS = 128, NGRP = SKV / GS;
constexpr int NWAVE = 4, NTHR = NWAVE * 64;
constexpr int NSPLIT = 64;
constexpr int NUNIT = NGRP * 4;
constexpr int UPI = NSPLIT * NWAVE;
constexpr int NIT = NUNIT / UPI;
constexpr int OSTR = 132;
constexpr int WBYTES = 16 * OSTR * 4;
constexpr size_t WS_PAY = 4096;
constexpr unsigned SPIN_CAP = 1u << 20;

typedef unsigned v4u __attribute__((ext_vector_type(4)));
typedef unsigned v2u __attribute__((ext_vector_type(2)));
typedef float f32x4 __attribute__((ext_vector_type(4)));
typedef float f32x2 __attribute__((ext_vector_type(2)));
typedef __bf16 bf16x8 __attribute__((ext_vector_type(8)));
typedef __bf16 bf16x2 __attribute__((ext_vector_type(2)));
#define LAS __attribute__((address_space(3)))
#define GAS __attribute__((address_space(1)))
typedef GAS unsigned gu32;
typedef GAS unsigned long long gu64;
#define RLX_AGENT __ATOMIC_RELAXED, __HIP_MEMORY_SCOPE_AGENT
#define RSRC(p, n) __builtin_amdgcn_make_buffer_rsrc((void*)(p), 0, (n), 0x00020000)

__device__ __forceinline__ unsigned pk_bf16(float lo, float hi) {
  f32x2 v = {lo, hi};
  return __builtin_bit_cast(unsigned, __builtin_convertvector(v, bf16x2));
}
__device__ __forceinline__ float bf_lo(unsigned p) { return __uint_as_float(p << 16); }
__device__ __forceinline__ float bf_hi(unsigned p) { return __uint_as_float(p & 0xffff0000u); }
__device__ __forceinline__ f32x4 mfma16(v4u a, v4u b, f32x4 c) {
  return __builtin_amdgcn_mfma_f32_16x16x32_bf16(__builtin_bit_cast(bf16x8, a), __builtin_bit_cast(bf16x8, b), c, 0, 0, 0);
}
__device__ __forceinline__ v4u ldnt(__amdgpu_buffer_rsrc_t rs, int voff, int soff) {
  return __builtin_amdgcn_raw_buffer_load_b128(rs, voff, soff, 2);
}
__device__ __forceinline__ v2u ldnt8(__amdgpu_buffer_rsrc_t rs, int voff, int soff) {
  return __builtin_amdgcn_raw_buffer_load_b64(rs, voff, soff, 2);
}
template <int CTRL> __device__ __forceinline__ float dpp(float x) {
  return __int_as_float(__builtin_amdgcn_mov_dpp(__float_as_int(x), CTRL, 0xf, 0xf, true));
}
constexpr int ROR1 = 0x121, ROR2 = 0x122, ROR4 = 0x124, ROR8 = 0x128;
__device__ __forceinline__ float xrow_max(float x) {
  auto s = __builtin_amdgcn_permlane16_swap(__float_as_uint(x), __float_as_uint(x), false, false);
  x = fmaxf(__uint_as_float(s[0]), __uint_as_float(s[1]));
  auto t = __builtin_amdgcn_permlane32_swap(__float_as_uint(x), __float_as_uint(x), false, false);
  return fmaxf(__uint_as_float(t[0]), __uint_as_float(t[1]));
}
__device__ __forceinline__ float xrow_sum(float x) {
  auto s = __builtin_amdgcn_permlane16_swap(__float_as_uint(x), __float_as_uint(x), false, false);
  x = __uint_as_float(s[0]) + __uint_as_float(s[1]);
  auto t = __builtin_amdgcn_permlane32_swap(__float_as_uint(x), __float_as_uint(x), false, false);
  return __uint_as_float(t[0]) + __uint_as_float(t[1]);
}
__device__ __forceinline__ float row_sum(float x) {
  x += dpp<ROR1>(x);
  x += dpp<ROR2>(x);
  x += dpp<ROR4>(x);
  return x + dpp<ROR8>(x);
}

template <int N> struct IntT { static constexpr int value = N; };
struct TrueT { static constexpr bool value = true; };
struct FalseT { static constexpr bool value = false; };
constexpr float LOG2E = 1.4426950408889634f;
constexpr float SCALE = 0.08838834764831845f;

__global__ void __launch_bounds__(NTHR, 2)
kv_decode(const unsigned short* __restrict__ q, const int* __restrict__ qk, const int* __restrict__ qv,
          const unsigned short* __restrict__ ks, const unsigned short* __restrict__ kz,
          const unsigned short* __restrict__ vs, const unsigned short* __restrict__ vz,
          unsigned char* ws, unsigned short* __restrict__ out) {
  __shared__ __attribute__((aligned(16))) unsigned char smem[NWAVE * WBYTES + NWAVE * 8 * 16 + NWAVE * NIT * 64 * 8 + LDS_PAD];
  const int tid = threadIdx.x, lane = tid & 63;
  unsigned long long pc0;
  asm volatile("s_getpc_b64 %0" : "=s"(pc0));
  const unsigned cw = __builtin_amdgcn_raw_buffer_load_b32(RSRC(pc0 & ~255ull, CODE_KB * 1024),
                                                           ((tid & 31) + 32 * ((blockIdx.x >> 3) & 7)) * 128 | (tid & 224) << 22, 0, 0);
  __builtin_amdgcn_sched_barrier(0);
  asm volatile("" ::"s"(qk), "s"(kz), "s"(vz), "s"(out));
  const int w = __builtin_amdgcn_readfirstlane(tid >> 6);
  const int hk = blockIdx.x & 3, cb = (blockIdx.x >> 3) + 32 * ((blockIdx.x >> 2) & 1);
  const int rot = ((blockIdx.x >> 3) + 2 * w) & 7;
  constexpr int HEADB = SKV * DH * 4;
  const char* kh = (const char*)qk + (size_t)hk * HEADB;
  const __amdgpu_buffer_rsrc_t rk = RSRC(kh, HEADB);
  const int voffK = lane * 16;
#define UBASE(j) (((j) >> 1) * 32768 + ((j) & 1) * 2048)
#define LDK(rs, gb, p) ldnt(rs, voffK + ((p) & 1) * 1024, (gb) + ((((p) >> 1) ^ rot) << 12))
#define LDV(rs, gb, n) ldnt(rs, (((n) & 4) ? voffV1 : voffV0) + ((n) & 3) * 512 + ((n) >> 3) * 256, (gb))
#define SOFF_S(j) (((j) >> 2) * 256)
#define SOFF_T(j) (((j) >> 1) * 128 + ((j) & 1) * 8)
  const int j0 = (cb * NWAVE + w + HSTAG * hk * UPI) & (NUNIT - 1);
  v4u a[16];
#pragma unroll
  for (int i = 0; i < KFIRST; ++i) a[i] = LDK(rk, UBASE(j0), i);
  __builtin_amdgcn_sched_barrier(0);
  unsigned due = (unsigned)__builtin_amdgcn_s_memrealtime() + T_UNIT1 - TAU;
  const __amdgpu_buffer_rsrc_t rks = RSRC(ks + (size_t)hk * NGRP * DH, NGRP * DH * 2);
  const __amdgpu_buffer_rsrc_t rkz = RSRC(kz + (size_t)hk * NGRP * DH, NGRP * DH * 2);
  const int voffS = 8 * (lane & 31);
  v2u kzr = ldnt8(rkz, voffS, SOFF_S(j0));
  v2u ksr = ldnt8(rks, voffS, SOFF_S(j0));
#pragma unroll
  for (int i = KFIRST; i < 8; ++i) a[i] = LDK(rk, UBASE(j0), i);
  __builtin_amdgcn_sched_barrier(0);
  const int g = lane >> 4, c = lane & 15, u = (lane >> 3) & 1, h = lane & 7;
  v4u qf[4];
#pragma unroll
  for (int kk = 0; kk < 4; ++kk) qf[kk] = __builtin_nontemporal_load((const v4u*)(q + (size_t)(hk * 8 + h) * DH + 32 * kk + 8 * g));
  __builtin_amdgcn_sched_barrier(0);
#pragma unroll
  for (int i = 8; i < BURST; ++i) a[i] = LDK(rk, UBASE(j0), i);
  __builtin_amdgcn_sched_barrier(0);
  const __amdgpu_buffer_rsrc_t rp = RSRC(ws + WS_PAY + (size_t)hk * NSPLIT * NSPLIT * 64, NSPLIT * NSPLIT * 64);
  unsigned tag = *(const volatile __attribute__((address_space(4))) unsigned*)(ws + hk * 256) + 0x01010101u;
  tag = tag ? tag : 0x01010101u;
  __builtin_amdgcn_sched_barrier(0);

  const __amdgpu_buffer_rsrc_t rv = RSRC((const char*)qv + (size_t)hk * HEADB, HEADB);
  const __amdgpu_buffer_rsrc_t rvs = RSRC(vs + (size_t)hk * SKV, SKV * 2);
  const __amdgpu_buffer_rsrc_t rvz = RSRC(vz + (size_t)hk * SKV, SKV * 2);
  const int voffV0 = ((g ^ rot) << 12) + c * 16, voffV1 = (((4 + g) ^ rot) << 12) + c * 16;
  const int voffT = 16 * ((4 * u + g) ^ rot);
  LAS unsigned char* kbuf = (LAS unsigned char*)smem + w * WBYTES;
  LAS f32x4* mbuf = (LAS f32x4*)((LAS unsigned char*)smem + NWAVE * WBYTES);
  LAS v2u* sbuf = (LAS v2u*)((LAS unsigned char*)smem + NWAVE * WBYTES + NWAVE * 8 * 16) + w * (NIT * 64) + lane;
  const int hi = lane >> 5;
  const int wr0 = hi * 256 + ((((lane & 31) >> 1) ^ hi) << 4) + 8 * (lane & 1);
  float m = -INFINITY;
  v2u vsr, vzr;
#define KSTAMP(it_) {}

#define SB() __builtin_amdgcn_sched_barrier(0)
  int ahead = 1;
#define WAITN(c, step) ((c) - 1 + (((step) > 4 && (step) <= 4 + (c)) ? 2 : 0))
  int late = -SLACK0;
#define THROTTLE(step) asm volatile("s_cmp_lt_i32 %0, %1\n\ts_cbranch_scc1 1f\n\ts_waitcnt vmcnt(%3)\n\ts_cmp_lt_i32 %0, 0\n\ts_cbranch_scc1 1f\n\ts_waitcnt vmcnt(%2)\n1:" \
                                    ::"s"(ahead), "s"(late), "i"(WAITN(CAP, step)), "i"(WAITN(CAP2, step)) : "scc")
#define NEXT_UNIT() do { due += TAU; late -= SLACK1; } while (0)
#define SCHED(step) do { if ((step) == 0) NEXT_UNIT(); \
    if ((step) % CHK == 0) ahead = (int)(due + (step) * TAU / 16 - (unsigned)__builtin_amdgcn_s_memrealtime()); } while (0)
  f32x4 d0, d1;
  v4u kf[4];
  auto frag_read = [&](int half) __attribute__((always_inline)) {
#pragma unroll
    for (int kk = 0; kk < 4; ++kk) kf[kk] = *(LAS v4u*)(kbuf + c * 256 + (((4 * kk + g) ^ c) << 4) + 4096 * half);
  };
  auto qk_mfma = [&]() __attribute__((always_inline)) {
    f32x4 d = {0.f, 0.f, 0.f, 0.f};
#pragma unroll
    for (int kk = 0; kk < 4; ++kk) d = mfma16(kf[kk], qf[kk], d);
    return d;
  };
  auto put_logits = [&](int it) __attribute__((always_inline)) {
    const unsigned r01 = pk_bf16(u ? d1[0] : d0[0], u ? d1[1] : d0[1]);
    const unsigned r23 = pk_bf16(u ? d1[2] : d0[2], u ? d1[3] : d0[3]);
    v2u sv;
    sv.x = pk_bf16(bf_lo(r01) * SCALE, bf_hi(r01) * SCALE);
    sv.y = pk_bf16(bf_lo(r23) * SCALE, bf_hi(r23) * SCALE);
    sbuf[it * 64] = sv;
    m = fmaxf(fmaxf(m, fmaxf(bf_lo(sv.x), bf_hi(sv.x))), fmaxf(bf_lo(sv.y), bf_hi(sv.y)));
    asm volatile("" ::"v"(m));
  };
  auto k_unit = [&](auto FIRST_, auto LAST_, int j, int it) __attribute__((always_inline)) {
    constexpr bool FIRST = decltype(FIRST_)::value, LAST = decltype(LAST_)::value;
    constexpr int ISSUED = FIRST ? BURST : DK;
    const int gb = UBASE(j);
    const int jn = LAST ? j0 : (j + UPI) & (NUNIT - 1);
    const int gbn = UBASE(jn);
    const float kz0 = bf_lo(kzr.x), kz1 = bf_hi(kzr.x), kz2 = bf_lo(kzr.y), kz3 = bf_hi(kzr.y);
    const float ks0 = bf_lo(ksr.x), ks1 = bf_hi(ksr.x), ks2 = bf_lo(ksr.y), ks3 = bf_hi(ksr.y);
    SB();
#pragma unroll
    for (int p = 0; p < 16; ++p) {
      const v4u x = a[p];
      asm volatile("" ::"v"(x));
      if (!FIRST) THROTTLE(p);
      else if (p + DK >= ISSUED) asm volatile("s_waitcnt vmcnt(%0)" ::"i"(CAP - 1 + (p + DK - CAP < 16 ? 2 : 0)));
      SB();
      if (p + DK < 16) { if (p + DK >= ISSUED) a[p + DK] = LDK(rk, gb, p + DK); }
      else if (!LAST) a[(p + DK) & 15] = LDK(rk, gbn, (p + DK) & 15);
      else if (p + DK - 16 < DV) a[(p + DK) & 15] = LDV(rv, gbn, (p + DK) & 15);
      if (p == 4) {
        if (LAST) { vsr = ldnt8(rvs, voffT, SOFF_T(jn)); vzr = ldnt8(rvz, voffT, SOFF_T(jn)); }
        else { kzr = ldnt8(rkz, voffS, SOFF_S(jn)); ksr = ldnt8(rks, voffS, SOFF_S(jn)); }
      }
      SB();
      if (!FIRST) SCHED(p);
      if (p == 0 && !(it & 1)) KSTAMP(it >> 1)
      if (!FIRST && p == 0) frag_read(1);
      if (p == 8) frag_read(0);
      const unsigned d01 = pk_bf16((float)(int)x.x - kz0, (float)(int)x.y - kz1);
      const unsigned d23 = pk_bf16((float)(int)x.z - kz2, (float)(int)x.w - kz3);
      v2u kk2;
      kk2.x = pk_bf16(bf_lo(d01) * ks0, bf_hi(d01) * ks1);
      kk2.y = pk_bf16(bf_lo(d23) * ks2, bf_hi(d23) * ks3);
      *(LAS v2u*)(kbuf + (wr0 ^ (32 * (p & 7))) + 512 * p) = kk2;
      if (!FIRST && p == 1) d1 = qk_mfma();
      if (!FIRST && p == 2) put_logits(it - 1);
      if (p == 9) d0 = qk_mfma();
      SB();
    }
  };
  asm volatile("" ::"v"(cw));
  k_unit(TrueT{}, FalseT{}, j0, 0);
#pragma nounroll
  for (int it = 1; it < NIT - 1; ++it) k_unit(FalseT{}, FalseT{}, (j0 + it * UPI) & (NUNIT - 1), it);
  k_unit(FalseT{}, TrueT{}, (j0 + (NIT - 1) * UPI) & (NUNIT - 1), NIT - 1);
#pragma unroll
  for (int n = DK; n < DV; ++n) a[n] = LDV(rv, UBASE(j0), n);
  SB();
  frag_read(1);
  d1 = qk_mfma();
  put_logits(NIT - 1);

  m = fmaxf(m, dpp<ROR8>(m));
  m = xrow_max(m);
  float lsum = 0.f, csum = 0.f;
  f32x4 acc[2][4];
#pragma unroll
  for (int hf = 0; hf < 2; ++hf)
#pragma unroll
    for (int e = 0; e < 4; ++e) acc[hf][e] = f32x4{0.f, 0.f, 0.f, 0.f};
  float wgt[NWAVE], cterm;
  unsigned sideb;
  const int hsh = 16 * (lane & 1);
  const unsigned tagb = ((tag >> hsh) & 0xffu) | (((tag >> hsh) & 0xff00u) << 8);
  auto v_unit = [&](auto LAST_, int j, int it) __attribute__((always_inline)) {
    constexpr bool LAST = decltype(LAST_)::value;
    const int gb = UBASE(j);
    const int jn = (j + UPI) & (NUNIT - 1);
    const int gbn = UBASE(jn);
    if (LAST) {
#pragma unroll
      for (int n = DV; n < 16; ++n) a[n] = LDV(rv, gb, n);
    }
    const v2u sv = sbuf[it * 64];
    SB();
    v4u pf, vf[4];
    float p0, p1, p2, p3;
#pragma unroll
    for (int n = 0; n < 16; ++n) {
      if (!LAST) {
        asm volatile("" ::"v"(a[n]));
        THROTTLE(n);
        SB();
        a[(n + DV) & 15] = LDV(rv, n + DV < 16 ? gb : gbn, (n + DV) & 15);
        SB();
        SCHED(n);
      }
      if (n == 0 && !(it & 1)) KSTAMP(4 + (it >> 1))
      if (n & 1) {
#pragma unroll
        for (int e = 0; e < 4; ++e) vf[e][(n >> 1) & 3] = pk_bf16((float)(int)a[n - 1][e], (float)(int)a[n][e]);
        asm volatile("" ::"v"(vf[0][(n >> 1) & 3]), "v"(vf[1][(n >> 1) & 3]), "v"(vf[2][(n >> 1) & 3]), "v"(vf[3][(n >> 1) & 3]));
      }
      if (n == 0) {
        p0 = __builtin_amdgcn_exp2f((bf_lo(sv.x) - m) * LOG2E); p1 = __builtin_amdgcn_exp2f((bf_hi(sv.x) - m) * LOG2E);
        p2 = __builtin_amdgcn_exp2f((bf_lo(sv.y) - m) * LOG2E); p3 = __builtin_amdgcn_exp2f((bf_hi(sv.y) - m) * LOG2E);
        lsum += (p0 + p1) + (p2 + p3);
        asm volatile("" ::"v"(lsum));
      }
      if (n == (LAST ? 0 : 2)) {
        const unsigned p01 = pk_bf16(p0 * bf_lo(vsr.x), p1 * bf_hi(vsr.x));
        const unsigned p23 = pk_bf16(p2 * bf_lo(vsr.y), p3 * bf_hi(vsr.y));
        csum = fmaf(bf_lo(p01), bf_lo(vzr.x), csum);
        csum = fmaf(bf_hi(p01), bf_hi(vzr.x), csum);
        csum = fmaf(bf_lo(p23), bf_lo(vzr.y), csum);
        csum = fmaf(bf_hi(p23), bf_hi(vzr.y), csum);
        pf.x = u ? 0u : p01;
        pf.y = u ? 0u : p23;
        pf.z = u ? p01 : 0u;
        pf.w = u ? p23 : 0u;
        asm volatile("" ::"v"(pf), "v"(csum));
      }
      if (!LAST && n == 4) {
        vsr = ldnt8(rvs, voffT, SOFF_T(jn));
        vzr = ldnt8(rvz, voffT, SOFF_T(jn));
      }
      if (LAST && n == 0) {
        const float lw = xrow_sum(lsum + dpp<ROR8>(lsum)), cw = xrow_sum(csum + dpp<ROR8>(csum));
        if (lane < 8) mbuf[w * 8 + h] = f32x4{m, lw, cw, 0.f};
        __syncthreads();
        f32x4 ml[NWAVE];
#pragma unroll
        for (int i = 0; i < NWAVE; ++i) ml[i] = mbuf[i * 8 + 2 * w + hi];
        const float M = fmaxf(fmaxf(ml[0][0], ml[1][0]), fmaxf(ml[2][0], ml[3][0]));
        float L = 0.f, C = 0.f;
#pragma unroll
        for (int i = 0; i < NWAVE; ++i) {
          wgt[i] = __builtin_amdgcn_exp2f((ml[i][0] - M) * LOG2E);
          L = fmaf(wgt[i], ml[i][1], L);
          C = fmaf(wgt[i], ml[i][2], C);
        }
        const float inv = __builtin_amdgcn_rcpf(L);
#pragma unroll
        for (int i = 0; i < NWAVE; ++i) wgt[i] *= inv;
        cterm = -C * inv;
        const unsigned lh = __float_as_uint(fmaf(M, LOG2E, __builtin_amdgcn_logf(L))) >> hsh;
        sideb = tagb | ((lh & 0xffu) << 8) | ((lh & 0xff00u) << 16);
        asm volatile("" ::"v"(wgt[0]), "v"(wgt[1]), "v"(wgt[2]), "v"(wgt[3]), "v"(cterm), "v"(sideb));
      }
      if (LAST && n == 10) {
#pragma unroll
        for (int e = 0; e < 4; ++e) *(LAS f32x4*)((LAS float*)kbuf + c * OSTR + 16 * g + 4 * e) = acc[0][e];
      }
      if ((n & 7) == 7) {
#pragma unroll
        for (int e = 0; e < 4; ++e) acc[n >> 3][e] = mfma16(vf[e], pf, acc[n >> 3][e]);
      }
      SB();
    }
  };
#pragma nounroll
  for (int it = 0; it < NIT - 1; ++it) v_unit(FalseT{}, (j0 + it * UPI) & (NUNIT - 1), it);
  v_unit(TrueT{}, (j0 + (NIT - 1) * UPI) & (NUNIT - 1), NIT - 1);
  {
    LAS float* obuf = (LAS float*)kbuf + c * OSTR + 16 * g;
#pragma unroll
    for (int hf = 1; hf < 2; ++hf)
#pragma unroll
      for (int e = 0; e < 4; ++e) *(LAS f32x4*)(obuf + 64 * hf + 4 * e) = acc[hf][e];
  }
  __syncthreads();
  {
    f32x4 ob[NWAVE][2];
#pragma unroll
    for (int i = 0; i < NWAVE; ++i) {
      const LAS float* op = (const LAS float*)((LAS unsigned char*)smem + i * WBYTES) + (2 * w + hi) * OSTR + 4 * (lane & 31);
      ob[i][0] = *(const LAS f32x4*)op;
      ob[i][1] = *(const LAS f32x4*)(op + 8 * OSTR);
    }
    f32x4 o = {cterm, cterm, cterm, cterm};
#pragma unroll
    for (int i = 0; i < NWAVE; ++i) o += wgt[i] * (ob[i][0] + ob[i][1]);
    const v4u pk = {(__float_as_uint(o[0]) & 0xffffff00u) | (sideb & 0xffu), (__float_as_uint(o[1]) & 0xffffff00u) | ((sideb >> 8) & 0xffu),
                    (__float_as_uint(o[2]) & 0xffffff00u) | ((sideb >> 16) & 0xffu), (__float_as_uint(o[3]) & 0xffffff00u) | (sideb >> 24)};
    const int cons = 16 * w + (lane >> 2);
    __builtin_amdgcn_raw_buffer_store_b128(pk, rp, ((cons >> 1) * NSPLIT + ((cb + (cons >> 1)) & (NSPLIT - 1))) * 128 + (lane & 7) * 16, 0, 16);
  }
  asm volatile("s_waitcnt vmcnt(0)" ::: "memory");
  __builtin_amdgcn_s_sleep(FIRST_SLEEP);
  {
    const int pr = 16 * w + (lane >> 2);
    constexpr int OOB = 0x40000000;
    int po = ((cb >> 1) * NSPLIT + pr) * 128 + (cb & 1) * 64 + (lane & 3) * 16;
    v4u xo = {0u, 0u, 0u, 0u};
    unsigned spins = 0;
    bool bad = false;
    while (true) {
      asm volatile("" ::: "memory");
      const v4u x = __builtin_amdgcn_raw_buffer_load_b128(rp, po, 0, 16);
      unsigned long long okm = __ballot(((x.x & 0xffu) | ((x.z & 0xffu) << 16)) == tagb);
      okm &= okm >> 1;
      okm &= okm >> 2;
      okm = (okm & 0x1111111111111111ull) * 15ull;
      const bool k = (okm >> lane) & 1ull;
      xo = k ? x : xo;
      po = k ? OOB : po;
      const int miss = __builtin_popcountll(__ballot(po != OOB));
      if (miss == 0) break;
      if (++spins > SPIN_CAP) { bad = true; break; }
      if (miss > 4 * FETCH_MISS) __builtin_amdgcn_s_sleep(SLOW_SLEEP);
      else if (POLL_SLEEP) __builtin_amdgcn_s_sleep(POLL_SLEEP);
    }
    const unsigned lhalf = ((xo.y & 0xffu) | ((xo.w & 0xffu) << 8)) << hsh;
    float lse = __uint_as_float(lhalf | (unsigned)__builtin_amdgcn_mov_dpp((int)lhalf, 0xB1, 0xf, 0xf, true));
    if (bad) lse = __builtin_nanf("");
    float M = fmaxf(lse, dpp<ROR4>(lse));
    M = fmaxf(M, dpp<ROR8>(M));
    M = xrow_max(M);
    const float e = __builtin_amdgcn_exp2f(lse - M);
    float t;
    {
      float av[4];
#pragma unroll
      for (int i = 0; i < 4; ++i) av[i] = e * __uint_as_float(xo[i] & 0xffffff00u);
      auto s02 = __builtin_amdgcn_permlane32_swap(__float_as_uint(av[0]), __float_as_uint(av[2]), false, false);
      auto s13 = __builtin_amdgcn_permlane32_swap(__float_as_uint(av[1]), __float_as_uint(av[3]), false, false);
      const float b0 = __uint_as_float(s02[0]) + __uint_as_float(s02[1]);
      const float b1 = __uint_as_float(s13[0]) + __uint_as_float(s13[1]);
      auto sw = __builtin_amdgcn_permlane16_swap(__float_as_uint(b0), __float_as_uint(b1), false, false);
      t = __uint_as_float(sw[0]) + __uint_as_float(sw[1]);
    }
    t += dpp<ROR4>(t);
    t += dpp<ROR8>(t);
    float L = e + dpp<ROR4>(e);
    L = xrow_sum(L + dpp<ROR8>(L));
    LAS f32x4* fin = (LAS f32x4*)((LAS unsigned char*)smem + NWAVE * WBYTES + NWAVE * 8 * 16);
    if (c < 4) fin[w * 16 + 4 * g + c] = f32x4{t, M, L, 0.f};
    __syncthreads();
    if (tid < 16) {
      f32x4 r[NWAVE];
#pragma unroll
      for (int i = 0; i < NWAVE; ++i) r[i] = fin[i * 16 + tid];
      const float Mx = fmaxf(fmaxf(r[0][1], r[1][1]), fmaxf(r[2][1], r[3][1]));
      float Lx = 0.f, ox = 0.f;
#pragma unroll
      for (int i = 0; i < NWAVE; ++i) {
        const float ee = __builtin_amdgcn_exp2f(r[i][1] - Mx);
        Lx = fmaf(ee, r[i][2], Lx);
        ox = fmaf(ee, r[i][0], ox);
      }
      out[(hk * 8 + (cb >> 3)) * DH + 16 * (cb & 7) + tid] = (unsigned short)pk_bf16(ox * __builtin_amdgcn_rcpf(Lx), 0.f);
      if (cb == 0 && tid == 0) __hip_atomic_store((gu32*)ws + hk * 64, tag, RLX_AGENT);
    }
  }
}
}

extern "C" void kernel_launch(void* const* d_in, const int* in_sizes, int n_in, void* d_out, int out_size, void* d_ws,
                              size_t ws_size, hipStream_t stream) {
  kv_decode<<<HKV * NSPLIT, NTHR, 0, stream>>>((const unsigned short*)d_in[0], (const int*)d_in[1], (const int*)d_in[2],
                                               (const unsigned short*)d_in[3], (const unsigned short*)d_in[4],
                                               (const unsigned short*)d_in[5], (const unsigned short*)d_in[6],
                                               (unsigned char*)d_ws, (unsigned short*)d_out);
}
```

```cpp
#define T_UNIT1 355
#define SLACK0 45
#define GATE 230
#define FIRST_SLEEP 4
#include <hip/hip_runtime.h>
#include <hip/hip_bf16.h>
#include <stdint.h>

#define DIST 8
#define CAP 5
#define CAP2 6
#define SLACK1 4
#define TAU 218
#define CODE_KB 18
#define DK DIST
#define DV DIST
#define BURST 16
#define KFIRST 4
#define QAT 8
#define CWAVE 0
#define CHK 4
#define HSTAG 2
#define POLL_SLEEP 2
#define SLOW_SLEEP 4
#define FETCH_MISS 4
#define LDS_PAD 0

namespace {
constexpr int HKV = 4, SKV = 65536, DH = 128, GS = 128, NGRP = SKV / GS;
constexpr int NWAVE = 4, NTHR = NWAVE * 64;
constexpr int NSPLIT = 64;
constexpr int NUNIT = NGRP * 4;
constexpr int UPI = NSPLIT * NWAVE;
constexpr int NIT = NUNIT / UPI;
constexpr int OSTR = 132;
constexpr int WBYTES = 16 * OSTR * 4;
constexpr size_t WS_PAY = 4096;
constexpr unsigned SPIN_CAP = 1u << 20;

typedef unsigned v4u __attribute__((ext_vector_type(4)));
typedef unsigned v2u __attribute__((ext_vector_type(2)));
typedef float f32x4 __attribute__((ext_vector_type(4)));
typedef float f32x2 __attribute__((ext_vector_type(2)));
typedef __bf16 bf16x8 __attribute__((ext_vector_type(8)));
typedef __bf16 bf16x2 __attribute__((ext_vector_type(2)));
#define LAS __attribute__((address_space(3)))
#define GAS __attribute__((address_space(1)))
typedef GAS unsigned gu32;
typedef GAS unsigned long long gu64;
#define RLX_AGENT __ATOMIC_RELAXED, __HIP_MEMORY_SCOPE_AGENT
#define RSRC(p, n) __builtin_amdgcn_make_buffer_rsrc((void*)(p), 0, (n), 0x00020000)

__device__ __forceinline__ unsigned pk_bf16(float lo, float hi) {
  f32x2 v = {lo, hi};
  return __builtin_bit_cast(unsigned, __builtin_convertvector(v, bf16x2));
}
__device__ __forceinline__ float bf_lo(unsigned p) { return __uint_as_float(p << 16); }
__device__ __forceinline__ float bf_hi(unsigned p) { return __uint_as_float(p & 0xffff0000u); }
__device__ __forceinline__ f32x4 mfma16(v4u a, v4u b, f32x4 c) {
  return __builtin_amdgcn_mfma_f32_16x16x32_bf16(__builtin_bit_cast(bf16x8, a), __builtin_bit_cast(bf16x8, b), c, 0, 0, 0);
}
__device__ __forceinline__ v4u ldnt(__amdgpu_buffer_rsrc_t rs, int voff, int soff) {
  return __builtin_amdgcn_raw_buffer_load_b128(rs, voff, soff, 2);
}
__device__ __forceinline__ v2u ldnt8(__amdgpu_buffer_rsrc_t rs, int voff, int soff) {
  return __builtin_amdgcn_raw_buffer_load_b64(rs, voff, soff, 2);
}
template <int CTRL> __device__ __forceinline__ float dpp(float x) {
  return __int_as_float(__builtin_amdgcn_mov_dpp(__float_as_int(x), CTRL, 0xf, 0xf, true));
}
constexpr int ROR1 = 0x121, ROR2 = 0x122, ROR4 = 0x124, ROR8 = 0x128;
__device__ __forceinline__ float xrow_max(float x) {
  auto s = __builtin_amdgcn_permlane16_swap(__float_as_uint(x), __float_as_uint(x), false, false);
  x = fmaxf(__uint_as_float(s[0]), __uint_as_float(s[1]));
  auto t = __builtin_amdgcn_permlane32_swap(__float_as_uint(x), __float_as_uint(x), false, false);
  return fmaxf(__uint_as_float(t[0]), __uint_as_float(t[1]));
}
__device__ __forceinline__ float xrow_sum(float x) {
  auto s = __builtin_amdgcn_permlane16_swap(__float_as_uint(x), __float_as_uint(x), false, false);
  x = __uint_as_float(s[0]) + __uint_as_float(s[1]);
  auto t = __builtin_amdgcn_permlane32_swap(__float_as_uint(x), __float_as_uint(x), false, false);
  return __uint_as_float(t[0]) + __uint_as_float(t[1]);
}
__device__ __forceinline__ float row_sum(float x) {
  x += dpp<ROR1>(x);
  x += dpp<ROR2>(x);
  x += dpp<ROR4>(x);
  return x + dpp<ROR8>(x);
}

template <int N> struct IntT { static constexpr int value = N; };
struct TrueT { static constexpr bool value = true; };
struct FalseT { static constexpr bool value = false; };
constexpr float LOG2E = 1.4426950408889634f;
constexpr float SCALE = 0.08838834764831845f;

__global__ void __launch_bounds__(NTHR, 2)
kv_decode(const unsigned short* __restrict__ q, const int* __restrict__ qk, const int* __restrict__ qv,
          const unsigned short* __restrict__ ks, const unsigned short* __restrict__ kz,
          const unsigned short* __restrict__ vs, const unsigned short* __restrict__ vz,
          unsigned char* ws, unsigned short* __restrict__ out) {
  __shared__ __attribute__((aligned(16))) unsigned char smem[NWAVE * WBYTES + NWAVE * 8 * 16 + NWAVE * NIT * 64 * 8 + LDS_PAD];
  const int tid = threadIdx.x, lane = tid & 63;
  unsigned long long pc0;
  asm volatile("s_getpc_b64 %0" : "=s"(pc0));
  const unsigned cw = __builtin_amdgcn_raw_buffer_load_b32(RSRC(pc0 & ~255ull, CODE_KB * 1024),
                                                           ((tid & 31) + 32 * ((blockIdx.x >> 3) & 7)) * 128 | ((tid ^ (CWAVE * 64)) & 224) << 22, 0, 0);
  __builtin_amdgcn_sched_barrier(0);
  asm volatile("" ::"s"(qk), "s"(kz), "s"(vz), "s"(out));
  const int w = __builtin_amdgcn_readfirstlane(tid >> 6);
  const int hk = blockIdx.x & 3, cb = (blockIdx.x >> 3) + 32 * ((blockIdx.x >> 2) & 1);
  const int rot = ((blockIdx.x >> 3) + 2 * w) & 7;
  constexpr int HEADB = SKV * DH * 4;
  const char* kh = (const char*)qk + (size_t)hk * HEADB;
  const __amdgpu_buffer_rsrc_t rk = RSRC(kh, HEADB);
  const int voffK = lane * 16;
#define UBASE(j) (((j) >> 1) * 32768 + ((j) & 1) * 2048)
#define LDK(rs, gb, p) ldnt(rs, voffK + ((p) & 1) * 1024, (gb) + ((((p) >> 1) ^ rot) << 12))
#define LDV(rs, gb, n) ldnt(rs, (((n) & 4) ? voffV1 : voffV0) + ((n) & 3) * 512 + ((n) >> 3) * 256, (gb))
#define SOFF_S(j) (((j) >> 2) * 256)
#define SOFF_T(j) (((j) >> 1) * 128 + ((j) & 1) * 8)
  const int j0 = (cb * NWAVE + w + HSTAG * hk * UPI) & (NUNIT - 1);
  v4u a[16];
#pragma unroll
  for (int i = 0; i < KFIRST; ++i) a[i] = LDK(rk, UBASE(j0), i);
  __builtin_amdgcn_sched_barrier(0);
  unsigned due = (unsigned)__builtin_amdgcn_s_memrealtime() + T_UNIT1 - TAU;
  const __amdgpu_buffer_rsrc_t rks = RSRC(ks + (size_t)hk * NGRP * DH, NGRP * DH * 2);
  const __amdgpu_buffer_rsrc_t rkz = RSRC(kz + (size_t)hk * NGRP * DH, NGRP * DH * 2);
  const int voffS = 8 * (lane & 31);
  v2u kzr = ldnt8(rkz, voffS, SOFF_S(j0));
  v2u ksr = ldnt8(rks, voffS, SOFF_S(j0));
#pragma unroll
  for (int i = KFIRST; i < QAT; ++i) a[i] = LDK(rk, UBASE(j0), i);
  __builtin_amdgcn_sched_barrier(0);
  const int g = lane >> 4, c = lane & 15, u = (lane >> 3) & 1, h = lane & 7;
  v4u qf[4];
#pragma unroll
  for (int kk = 0; kk < 4; ++kk) qf[kk] = __builtin_nontemporal_load((const v4u*)(q + (size_t)(hk * 8 + h) * DH + 32 * kk + 8 * g));
  __builtin_amdgcn_sched_barrier(0);
#pragma unroll
  for (int i = QAT; i < BURST; ++i) a[i] = LDK(rk, UBASE(j0), i);
  __builtin_amdgcn_sched_barrier(0);
  const __amdgpu_buffer_rsrc_t rp = RSRC(ws + WS_PAY + (size_t)hk * NSPLIT * NSPLIT * 64, NSPLIT * NSPLIT * 64);
  unsigned tag = *(const volatile __attribute__((address_space(4))) unsigned*)(ws + hk * 256) + 0x01010101u;
  tag = tag ? tag : 0x01010101u;
  __builtin_amdgcn_sched_barrier(0);

  const __amdgpu_buffer_rsrc_t rv = RSRC((const char*)qv + (size_t)hk * HEADB, HEADB);
  const __amdgpu_buffer_rsrc_t rvs = RSRC(vs + (size_t)hk * SKV, SKV * 2);
  const __amdgpu_buffer_rsrc_t rvz = RSRC(vz + (size_t)hk * SKV, SKV * 2);
  const int voffV0 = ((g ^ rot) << 12) + c * 16, voffV1 = (((4 + g) ^ rot) << 12) + c * 16;
  const int voffT = 16 * ((4 * u + g) ^ rot);
  LAS unsigned char* kbuf = (LAS unsigned char*)smem + w * WBYTES;
  LAS f32x4* mbuf = (LAS f32x4*)((LAS unsigned char*)smem + NWAVE * WBYTES);
  LAS v2u* sbuf = (LAS v2u*)((LAS unsigned char*)smem + NWAVE * WBYTES + NWAVE * 8 * 16) + w * (NIT * 64) + lane;
  const int hi = lane >> 5;
  const int wr0 = hi * 256 + ((((lane & 31) >> 1) ^ hi) << 4) + 8 * (lane & 1);
  float m = -INFINITY;
  v2u vsr, vzr;
#define KSTAMP(it_) {}

#define SB() __builtin_amdgcn_sched_barrier(0)
  int ahead = 1;
#define WAITN(c, step) ((c) - 1 + (((step) > 4 && (step) <= 4 + (c)) ? 2 : 0))
  int late = -SLACK0;
#define THROTTLE(step) asm volatile("s_cmp_lt_i32 %0, %1\n\ts_cbranch_scc1 1f\n\ts_waitcnt vmcnt(%3)\n\ts_cmp_lt_i32 %0, 0\n\ts_cbranch_scc1 1f\n\ts_waitcnt vmcnt(%2)\n1:" \
                                    ::"s"(ahead), "s"(late), "i"(WAITN(CAP, step)), "i"(WAITN(CAP2, step)) : "scc")
#define NEXT_UNIT() do { due += TAU; late -= SLACK1; } while (0)
#define SCHED(step) do { if ((step) == 0) NEXT_UNIT(); \
    if ((step) % CHK == 0) ahead = (int)(due + (step) * TAU / 16 - (unsigned)__builtin_amdgcn_s_memrealtime()); } while (0)
  f32x4 d0, d1;
  v4u kf[4];
  auto frag_read = [&](int half) __attribute__((always_inline)) {
#pragma unroll
    for (int kk = 0; kk < 4; ++kk) kf[kk] = *(LAS v4u*)(kbuf + c * 256 + (((4 * kk + g) ^ c) << 4) + 4096 * half);
  };
  auto qk_mfma = [&]() __attribute__((always_inline)) {
    f32x4 d = {0.f, 0.f, 0.f, 0.f};
#pragma unroll
    for (int kk = 0; kk < 4; ++kk) d = mfma16(kf[kk], qf[kk], d);
    return d;
  };
  auto put_logits = [&](int it) __attribute__((always_inline)) {
    const unsigned r01 = pk_bf16(u ? d1[0] : d0[0], u ? d1[1] : d0[1]);
    const unsigned r23 = pk_bf16(u ? d1[2] : d0[2], u ? d1[3] : d0[3]);
    v2u sv;
    sv.x = pk_bf16(bf_lo(r01) * SCALE, bf_hi(r01) * SCALE);
    sv.y = pk_bf16(bf_lo(r23) * SCALE, bf_hi(r23) * SCALE);
    sbuf[it * 64] = sv;
    m = fmaxf(fmaxf(m, fmaxf(bf_lo(sv.x), bf_hi(sv.x))), fmaxf(bf_lo(sv.y), bf_hi(sv.y)));
    asm volatile("" ::"v"(m));
  };
  auto k_unit = [&](auto FIRST_, auto LAST_, int j, int it) __attribute__((always_inline)) {
    constexpr bool FIRST = decltype(FIRST_)::value, LAST = decltype(LAST_)::value;
    constexpr int ISSUED = FIRST ? BURST : DK;
    const int gb = UBASE(j);
    const int jn = LAST ? j0 : (j + UPI) & (NUNIT - 1);
    const int gbn = UBASE(jn);
    const float kz0 = bf_lo(kzr.x), kz1 = bf_hi(kzr.x), kz2 = bf_lo(kzr.y), kz3 = bf_hi(kzr.y);
    const float ks0 = bf_lo(ksr.x), ks1 = bf_hi(ksr.x), ks2 = bf_lo(ksr.y), ks3 = bf_hi(ksr.y);
    SB();
#pragma unroll
    for (int p = 0; p < 16; ++p) {
      const v4u x = a[p];
      asm volatile("" ::"v"(x));
      if (!FIRST) THROTTLE(p);
      else if (p + DK >= ISSUED) asm volatile("s_waitcnt vmcnt(%0)" ::"i"(CAP - 1 + (p + DK - CAP < 16 ? 2 + (KFIRST == 16 ? 2 : 0) + (QAT == 16 ? 4 : 0) : 0)));
      SB();
      if (p + DK < 16) { if (p + DK >= ISSUED) a[p + DK] = LDK(rk, gb, p + DK); }
      else if (!LAST) a[(p + DK) & 15] = LDK(rk, gbn, (p + DK) & 15);
      else if (p + DK - 16 < DV) a[(p + DK) & 15] = LDV(rv, gbn, (p + DK) & 15);
      if (p == 4) {
        if (LAST) { vsr = ldnt8(rvs, voffT, SOFF_T(jn)); vzr = ldnt8(rvz, voffT, SOFF_T(jn)); }
        else { kzr = ldnt8(rkz, voffS, SOFF_S(jn)); ksr = ldnt8(rks, voffS, SOFF_S(jn)); }
      }
      SB();
      if (!FIRST) SCHED(p);
      if (p == 0 && !(it & 1)) KSTAMP(it >> 1)
      if (!FIRST && p == 0) frag_read(1);
      if (p == 8) frag_read(0);
      const unsigned d01 = pk_bf16((float)(int)x.x - kz0, (float)(int)x.y - kz1);
      const unsigned d23 = pk_bf16((float)(int)x.z - kz2, (float)(int)x.w - kz3);
      v2u kk2;
      kk2.x = pk_bf16(bf_lo(d01) * ks0, bf_hi(d01) * ks1);
      kk2.y = pk_bf16(bf_lo(d23) * ks2, bf_hi(d23) * ks3);
      *(LAS v2u*)(kbuf + (wr0 ^ (32 * (p & 7))) + 512 * p) = kk2;
      if (!FIRST && p == 1) d1 = qk_mfma();
      if (!FIRST && p == 2) put_logits(it - 1);
      if (p == 9) d0 = qk_mfma();
      SB();
    }
  };
  asm volatile("" ::"v"(cw));
  k_unit(TrueT{}, FalseT{}, j0, 0);
#pragma nounroll
  for (int it = 1; it < NIT - 1; ++it) k_unit(FalseT{}, FalseT{}, (j0 + it * UPI) & (NUNIT - 1), it);
  k_unit(FalseT{}, TrueT{}, (j0 + (NIT - 1) * UPI) & (NUNIT - 1), NIT - 1);
#pragma unroll
  for (int n = DK; n < DV; ++n) a[n] = LDV(rv, UBASE(j0), n);
  SB();
  frag_read(1);
  d1 = qk_mfma();
  put_logits(NIT - 1);

  m = fmaxf(m, dpp<ROR8>(m));
  m = xrow_max(m);
  float lsum = 0.f, csum = 0.f;
  f32x4 acc[2][4];
#pragma unroll
  for (int hf = 0; hf < 2; ++hf)
#pragma unroll
    for (int e = 0; e < 4; ++e) acc[hf][e] = f32x4{0.f, 0.f, 0.f, 0.f};
  float wgt[NWAVE], cterm;
  unsigned sideb;
  const int hsh = 16 * (lane & 1);
  const unsigned tagb = ((tag >> hsh) & 0xffu) | (((tag >> hsh) & 0xff00u) << 8);
  auto v_unit = [&](auto LAST_, int j, int it) __attribute__((always_inline)) {
    constexpr bool LAST = decltype(LAST_)::value;
    const int gb = UBASE(j);
    const int jn = (j + UPI) & (NUNIT - 1);
    const int gbn = UBASE(jn);
    if (LAST) {
#pragma unroll
      for (int n = DV; n < 16; ++n) a[n] = LDV(rv, gb, n);
    }
    const v2u sv = sbuf[it * 64];
    SB();
    v4u pf, vf[4];
    float p0, p1, p2, p3;
#pragma unroll
    for (int n = 0; n < 16; ++n) {
      if (!LAST) {
        asm volatile("" ::"v"(a[n]));
        THROTTLE(n);
        SB();
        a[(n + DV) & 15] = LDV(rv, n + DV < 16 ? gb : gbn, (n + DV) & 15);
        SB();
        SCHED(n);
      }
      if (n == 0 && !(it & 1)) KSTAMP(4 + (it >> 1))
      if (n & 1) {
#pragma unroll
        for (int e = 0; e < 4; ++e) vf[e][(n >> 1) & 3] = pk_bf16((float)(int)a[n - 1][e], (float)(int)a[n][e]);
        asm volatile("" ::"v"(vf[0][(n >> 1) & 3]), "v"(vf[1][(n >> 1) & 3]), "v"(vf[2][(n >> 1) & 3]), "v"(vf[3][(n >> 1) & 3]));
      }
      if (n == 0) {
        p0 = __builtin_amdgcn_exp2f((bf_lo(sv.x) - m) * LOG2E); p1 = __builtin_amdgcn_exp2f((bf_hi(sv.x) - m) * LOG2E);
        p2 = __builtin_amdgcn_exp2f((bf_lo(sv.y) - m) * LOG2E); p3 = __builtin_amdgcn_exp2f((bf_hi(sv.y) - m) * LOG2E);
        lsum += (p0 + p1) + (p2 + p3);
        asm volatile("" ::"v"(lsum));
      }
      if (n == (LAST ? 0 : 2)) {
        const unsigned p01 = pk_bf16(p0 * bf_lo(vsr.x), p1 * bf_hi(vsr.x));
        const unsigned p23 = pk_bf16(p2 * bf_lo(vsr.y), p3 * bf_hi(vsr.y));
        csum = fmaf(bf_lo(p01), bf_lo(vzr.x), csum);
        csum = fmaf(bf_hi(p01), bf_hi(vzr.x), csum);
        csum = fmaf(bf_lo(p23), bf_lo(vzr.y), csum);
        csum = fmaf(bf_hi(p23), bf_hi(vzr.y), csum);
        pf.x = u ? 0u : p01;
        pf.y = u ? 0u : p23;
        pf.z = u ? p01 : 0u;
        pf.w = u ? p23 : 0u;
        asm volatile("" ::"v"(pf), "v"(csum));
      }
      if (!LAST && n == 4) {
        vsr = ldnt8(rvs, voffT, SOFF_T(jn));
        vzr = ldnt8(rvz, voffT, SOFF_T(jn));
      }
      if (LAST && n == 0) {
        const float lw = xrow_sum(lsum + dpp<ROR8>(lsum)), cw = xrow_sum(csum + dpp<ROR8>(csum));
        if (lane < 8) mbuf[w * 8 + h] = f32x4{m, lw, cw, 0.f};
        __syncthreads();
        f32x4 ml[NWAVE];
#pragma unroll
        for (int i = 0; i < NWAVE; ++i) ml[i] = mbuf[i * 8 + 2 * w + hi];
        const float M = fmaxf(fmaxf(ml[0][0], ml[1][0]), fmaxf(ml[2][0], ml[3][0]));
        float L = 0.f, C = 0.f;
#pragma unroll
        for (int i = 0; i < NWAVE; ++i) {
          wgt[i] = __builtin_amdgcn_exp2f((ml[i][0] - M) * LOG2E);
          L = fmaf(wgt[i], ml[i][1], L);
          C = fmaf(wgt[i], ml[i][2], C);
        }
        const float inv = __builtin_amdgcn_rcpf(L);
#pragma unroll
        for (int i = 0; i < NWAVE; ++i) wgt[i] *= inv;
        cterm = -C * inv;
        const unsigned lh = __float_as_uint(fmaf(M, LOG2E, __builtin_amdgcn_logf(L))) >> hsh;
        sideb = tagb | ((lh & 0xffu) << 8) | ((lh & 0xff00u) << 16);
        asm volatile("" ::"v"(wgt[0]), "v"(wgt[1]), "v"(wgt[2]), "v"(wgt[3]), "v"(cterm), "v"(sideb));
      }
      if (LAST && n == 10) {
#pragma unroll
        for (int e = 0; e < 4; ++e) *(LAS f32x4*)((LAS float*)kbuf + c * OSTR + 16 * g + 4 * e) = acc[0][e];
      }
      if ((n & 7) == 7) {
#pragma unroll
        for (int e = 0; e < 4; ++e) acc[n >> 3][e] = mfma16(vf[e], pf, acc[n >> 3][e]);
      }
      SB();
    }
  };
#pragma nounroll
  for (int it = 0; it < NIT - 1; ++it) v_unit(FalseT{}, (j0 + it * UPI) & (NUNIT - 1), it);
  v_unit(TrueT{}, (j0 + (NIT - 1) * UPI) & (NUNIT - 1), NIT - 1);
  {
    LAS float* obuf = (LAS float*)kbuf + c * OSTR + 16 * g;
#pragma unroll
    for (int hf = 1; hf < 2; ++hf)
#pragma unroll
      for (int e = 0; e < 4; ++e) *(LAS f32x4*)(obuf + 64 * hf + 4 * e) = acc[hf][e];
  }
  __syncthreads();
  {
    f32x4 ob[NWAVE][2];
#pragma unroll
    for (int i = 0; i < NWAVE; ++i) {
      const LAS float* op = (const LAS float*)((LAS unsigned char*)smem + i * WBYTES) + (2 * w + hi) * OSTR + 4 * (lane & 31);
      ob[i][0] = *(const LAS f32x4*)op;
      ob[i][1] = *(const LAS f32x4*)(op + 8 * OSTR);
    }
    f32x4 o = {cterm, cterm, cterm, cterm};
#pragma unroll
    for (int i = 0; i < NWAVE; ++i) o += wgt[i] * (ob[i][0] + ob[i][1]);
    const v4u pk = {(__float_as_uint(o[0]) & 0xffffff00u) | (sideb & 0xffu), (__float_as_uint(o[1]) & 0xffffff00u) | ((sideb >> 8) & 0xffu),
                    (__float_as_uint(o[2]) & 0xffffff00u) | ((sideb >> 16) & 0xffu), (__float_as_uint(o[3]) & 0xffffff00u) | (sideb >> 24)};
    const int cons = 16 * w + (lane >> 2);
    __builtin_amdgcn_raw_buffer_store_b128(pk, rp, ((cons >> 1) * NSPLIT + ((cb + (cons >> 1)) & (NSPLIT - 1))) * 128 + (lane & 7) * 16, 0, 16);
  }
  asm volatile("s_waitcnt vmcnt(0)" ::: "memory");
  __builtin_amdgcn_s_sleep(FIRST_SLEEP);
  while ((int)(due + TAU + GATE - (unsigned)__builtin_amdgcn_s_memrealtime()) > 0) __builtin_amdgcn_s_sleep(1);
  {
    const int pr = 16 * w + (lane >> 2);
    constexpr int OOB = 0x40000000;
    int po = ((cb >> 1) * NSPLIT + pr) * 128 + (cb & 1) * 64 + (lane & 3) * 16;
    v4u xo = {0u, 0u, 0u, 0u};
    unsigned spins = 0;
    bool bad = false;
    while (true) {
      asm volatile("" ::: "memory");
      const v4u x = __builtin_amdgcn_raw_buffer_load_b128(rp, po, 0, 16);
      unsigned long long okm = __ballot(((x.x & 0xffu) | ((x.z & 0xffu) << 16)) == tagb);
      okm &= okm >> 1;
      okm &= okm >> 2;
      okm = (okm & 0x1111111111111111ull) * 15ull;
      const bool k = (okm >> lane) & 1ull;
      xo = k ? x : xo;
      po = k ? OOB : po;
      const int miss = __builtin_popcountll(__ballot(po != OOB));
      if (miss == 0) break;
      if (++spins > SPIN_CAP) { bad = true; break; }
      if (miss > 4 * FETCH_MISS) __builtin_amdgcn_s_sleep(SLOW_SLEEP);
      else if (POLL_SLEEP) __builtin_amdgcn_s_sleep(POLL_SLEEP);
    }
    const unsigned lhalf = ((xo.y & 0xffu) | ((xo.w & 0xffu) << 8)) << hsh;
    float lse = __uint_as_float(lhalf | (unsigned)__builtin_amdgcn_mov_dpp((int)lhalf, 0xB1, 0xf, 0xf, true));
    if (bad) lse = __builtin_nanf("");
    float M = fmaxf(lse, dpp<ROR4>(lse));
    M = fmaxf(M, dpp<ROR8>(M));
    M = xrow_max(M);
    const float e = __builtin_amdgcn_exp2f(lse - M);
    float t;
    {
      float av[4];
#pragma unroll
      for (int i = 0; i < 4; ++i) av[i] = e * __uint_as_float(xo[i] & 0xffffff00u);
      auto s02 = __builtin_amdgcn_permlane32_swap(__float_as_uint(av[0]), __float_as_uint(av[2]), false, false);
      auto s13 = __builtin_amdgcn_permlane32_swap(__float_as_uint(av[1]), __float_as_uint(av[3]), false, false);
      const float b0 = __uint_as_float(s02[0]) + __uint_as_float(s02[1]);
      const float b1 = __uint_as_float(s13[0]) + __uint_as_float(s13[1]);
      auto sw = __builtin_amdgcn_permlane16_swap(__float_as_uint(b0), __float_as_uint(b1), false, false);
      t = __uint_as_float(sw[0]) + __uint_as_float(sw[1]);
    }
    t += dpp<ROR4>(t);
    t += dpp<ROR8>(t);
    float L = e + dpp<ROR4>(e);
    L = xrow_sum(L + dpp<ROR8>(L));
    LAS f32x4* fin = (LAS f32x4*)((LAS unsigned char*)smem + NWAVE * WBYTES + NWAVE * 8 * 16);
    if (c < 4) fin[w * 16 + 4 * g + c] = f32x4{t, M, L, 0.f};
    __syncthreads();
    if (tid < 16) {
      f32x4 r[NWAVE];
#pragma unroll
      for (int i = 0; i < NWAVE; ++i) r[i] = fin[i * 16 + tid];
      const float Mx = fmaxf(fmaxf(r[0][1], r[1][1]), fmaxf(r[2][1], r[3][1]));
      float Lx = 0.f, ox = 0.f;
#pragma unroll
      for (int i = 0; i < NWAVE; ++i) {
        const float ee = __builtin_amdgcn_exp2f(r[i][1] - Mx);
        Lx = fmaf(ee, r[i][2], Lx);
        ox = fmaf(ee, r[i][0], ox);
      }
      out[(hk * 8 + (cb >> 3)) * DH + 16 * (cb & 7) + tid] = (unsigned short)pk_bf16(ox * __builtin_amdgcn_rcpf(Lx), 0.f);
      if (cb == 0 && tid == 0) __hip_atomic_store((gu32*)ws + hk * 64, tag, RLX_AGENT);
    }
  }
}
}

extern "C" void kernel_launch(void* const* d_in, const int* in_sizes, int n_in, void* d_out, int out_size, void* d_ws,
                              size_t ws_size, hipStream_t stream) {
  kv_decode<<<HKV * NSPLIT, NTHR, 0, stream>>>((const unsigned short*)d_in[0], (const int*)d_in[1], (const int*)d_in[2],
                                               (const unsigned short*)d_in[3], (const unsigned short*)d_in[4],
                                               (const unsigned short*)d_in[5], (const unsigned short*)d_in[6],
                                               (unsigned char*)d_ws, (unsigned short*)d_out);
}
```

```cpp
#include <hip/hip_runtime.h>
#include <hip/hip_bf16.h>
#include <stdint.h>

namespace {
constexpr int HKV = 4, SKV = 65536, DH = 128, GS = 128, NGRP = SKV / GS;
constexpr int NWAVE = 4, NTHR = NWAVE * 64;
constexpr int NSPLIT = 64;
constexpr int NUNIT = NGRP * 4;
constexpr int UPI = NSPLIT * NWAVE;
constexpr int NIT = NUNIT / UPI;
constexpr int OSTR = 132;
constexpr int WBYTES = 16 * OSTR * 4;
constexpr size_t WS_PAY = 4096;
constexpr unsigned SPIN_CAP = 1u << 20;
constexpr int OOB = 0x40000000;
constexpr int DIST = 8;
constexpr int CAP1 = 5, CAP2 = 6;
constexpr int T_UNIT1 = 355, TAU = 218;
constexpr int SLACK0 = 45, SLACK1 = 4;
constexpr int GATE = 230;
constexpr int CODE_KB = 18;

typedef unsigned v4u __attribute__((ext_vector_type(4)));
typedef unsigned v2u __attribute__((ext_vector_type(2)));
typedef float f32x4 __attribute__((ext_vector_type(4)));
typedef float f32x2 __attribute__((ext_vector_type(2)));
typedef __bf16 bf16x8 __attribute__((ext_vector_type(8)));
typedef __bf16 bf16x2 __attribute__((ext_vector_type(2)));
#define LAS __attribute__((address_space(3)))
typedef __attribute__((address_space(1))) unsigned gu32;
#define RSRC(p, n) __builtin_amdgcn_make_buffer_rsrc((void*)(p), 0, (n), 0x00020000)
constexpr int AUX_NT = 2, AUX_SC1 = 16;
#define SB() __builtin_amdgcn_sched_barrier(0)
#define NOW() ((unsigned)__builtin_amdgcn_s_memrealtime())

__device__ __forceinline__ unsigned pk_bf16(float lo, float hi) {
  f32x2 v = {lo, hi};
  return __builtin_bit_cast(unsigned, __builtin_convertvector(v, bf16x2));
}
__device__ __forceinline__ float bf_lo(unsigned p) { return __uint_as_float(p << 16); }
__device__ __forceinline__ float bf_hi(unsigned p) { return __uint_as_float(p & 0xffff0000u); }
__device__ __forceinline__ f32x4 mfma16(v4u a, v4u b, f32x4 c) {
  return __builtin_amdgcn_mfma_f32_16x16x32_bf16(__builtin_bit_cast(bf16x8, a), __builtin_bit_cast(bf16x8, b), c, 0, 0, 0);
}
__device__ __forceinline__ v4u ldnt(__amdgpu_buffer_rsrc_t rs, int voff, int soff) {
  return __builtin_amdgcn_raw_buffer_load_b128(rs, voff, soff, AUX_NT);
}
__device__ __forceinline__ v2u ldnt8(__amdgpu_buffer_rsrc_t rs, int voff, int soff) {
  return __builtin_amdgcn_raw_buffer_load_b64(rs, voff, soff, AUX_NT);
}
template <int CTRL> __device__ __forceinline__ float dpp(float x) {
  return __int_as_float(__builtin_amdgcn_mov_dpp(__float_as_int(x), CTRL, 0xf, 0xf, true));
}
constexpr int ROR4 = 0x124, ROR8 = 0x128, XOR1 = 0xB1;
__device__ __forceinline__ float xrow_max(float x) {
  auto s = __builtin_amdgcn_permlane16_swap(__float_as_uint(x), __float_as_uint(x), false, false);
  x = fmaxf(__uint_as_float(s[0]), __uint_as_float(s[1]));
  auto t = __builtin_amdgcn_permlane32_swap(__float_as_uint(x), __float_as_uint(x), false, false);
  return fmaxf(__uint_as_float(t[0]), __uint_as_float(t[1]));
}
__device__ __forceinline__ float xrow_sum(float x) {
  auto s = __builtin_amdgcn_permlane16_swap(__float_as_uint(x), __float_as_uint(x), false, false);
  x = __uint_as_float(s[0]) + __uint_as_float(s[1]);
  auto t = __builtin_amdgcn_permlane32_swap(__float_as_uint(x), __float_as_uint(x), false, false);
  return __uint_as_float(t[0]) + __uint_as_float(t[1]);
}
__device__ __forceinline__ int ubase(int j) { return (j >> 1) * 32768 + (j & 1) * 2048; }
__device__ __forceinline__ int soff_k(int j) { return (j >> 2) * 256; }
__device__ __forceinline__ int soff_v(int j) { return (j >> 1) * 128 + (j & 1) * 8; }
constexpr int waitn(int c, int step) { return c - 1 + ((step > 4 && step <= 4 + c) ? 2 : 0); }

struct TrueT { static constexpr bool value = true; };
struct FalseT { static constexpr bool value = false; };
constexpr float LOG2E = 1.4426950408889634f;
constexpr float SCALE = 0.08838834764831845f;

__global__ void __launch_bounds__(NTHR, 2)
kv_decode(const unsigned short* __restrict__ q, const int* __restrict__ qk, const int* __restrict__ qv,
          const unsigned short* __restrict__ ks, const unsigned short* __restrict__ kz,
          const unsigned short* __restrict__ vs, const unsigned short* __restrict__ vz,
          unsigned char* ws, unsigned short* __restrict__ out) {
  __shared__ __attribute__((aligned(16))) unsigned char smem[NWAVE * WBYTES + NWAVE * 8 * 16 + NWAVE * NIT * 64 * 8];
  const int tid = threadIdx.x, lane = tid & 63;
  unsigned long long pc0;
  asm volatile("s_getpc_b64 %0" : "=s"(pc0));
  const unsigned codew = __builtin_amdgcn_raw_buffer_load_b32(
      RSRC(pc0 & ~255ull, CODE_KB * 1024), ((tid & 31) + 32 * ((blockIdx.x >> 3) & 7)) * 128 | (tid & 224) << 22, 0, 0);
  SB();
  asm volatile("" ::"s"(qk), "s"(kz), "s"(vz), "s"(out));
  const int w = __builtin_amdgcn_readfirstlane(tid >> 6);
  const int hk = blockIdx.x & 3, cb = (blockIdx.x >> 3) + 32 * ((blockIdx.x >> 2) & 1);
  const int rot = ((blockIdx.x >> 3) + 2 * w) & 7;
  constexpr int HEADB = SKV * DH * 4;
  const __amdgpu_buffer_rsrc_t rk = RSRC((const char*)qk + (size_t)hk * HEADB, HEADB);
  const int voffK = lane * 16;
#define LDK(gb, p) ldnt(rk, voffK + ((p) & 1) * 1024, (gb) + ((((p) >> 1) ^ rot) << 12))
#define LDV(gb, n) ldnt(rv, (((n) & 4) ? voffV1 : voffV0) + ((n) & 3) * 512 + ((n) >> 3) * 256, (gb))
  const int j0 = (cb * NWAVE + w + 2 * hk * UPI) & (NUNIT - 1);
  v4u a[16];
#pragma unroll
  for (int i = 0; i < 4; ++i) a[i] = LDK(ubase(j0), i);
  SB();
  unsigned due = NOW() + T_UNIT1 - TAU;
  const __amdgpu_buffer_rsrc_t rks = RSRC(ks + (size_t)hk * NGRP * DH, NGRP * DH * 2);
  const __amdgpu_buffer_rsrc_t rkz = RSRC(kz + (size_t)hk * NGRP * DH, NGRP * DH * 2);
  const int voffS = 8 * (lane & 31);
  v2u kzr = ldnt8(rkz, voffS, soff_k(j0));
  v2u ksr = ldnt8(rks, voffS, soff_k(j0));
#pragma unroll
  for (int i = 4; i < 8; ++i) a[i] = LDK(ubase(j0), i);
  SB();
  const int g = lane >> 4, c = lane & 15, u = (lane >> 3) & 1, h = lane & 7, hi = lane >> 5;
  v4u qf[4];
#pragma unroll
  for (int kk = 0; kk < 4; ++kk) qf[kk] = __builtin_nontemporal_load((const v4u*)(q + (size_t)(hk * 8 + h) * DH + 32 * kk + 8 * g));
  SB();
#pragma unroll
  for (int i = 8; i < 16; ++i) a[i] = LDK(ubase(j0), i);
  SB();
  unsigned tag = *(const volatile __attribute__((address_space(4))) unsigned*)(ws + hk * 256) + 0x01010101u;
  tag = tag ? tag : 0x01010101u;
  SB();

  const __amdgpu_buffer_rsrc_t rp = RSRC(ws + WS_PAY + (size_t)hk * NSPLIT * NSPLIT * 64, NSPLIT * NSPLIT * 64);
  const __amdgpu_buffer_rsrc_t rv = RSRC((const char*)qv + (size_t)hk * HEADB, HEADB);
  const __amdgpu_buffer_rsrc_t rvs = RSRC(vs + (size_t)hk * SKV, SKV * 2);
  const __amdgpu_buffer_rsrc_t rvz = RSRC(vz + (size_t)hk * SKV, SKV * 2);
  const int voffV0 = ((g ^ rot) << 12) + c * 16, voffV1 = (((4 + g) ^ rot) << 12) + c * 16;
  const int voffT = 16 * ((4 * u + g) ^ rot);
  LAS unsigned char* kbuf = (LAS unsigned char*)smem + w * WBYTES;
  LAS f32x4* mbuf = (LAS f32x4*)((LAS unsigned char*)smem + NWAVE * WBYTES);
  LAS v2u* sbuf = (LAS v2u*)((LAS unsigned char*)smem + NWAVE * WBYTES + NWAVE * 8 * 16) + w * (NIT * 64) + lane;
  const int wr0 = hi * 256 + ((((lane & 31) >> 1) ^ hi) << 4) + 8 * (lane & 1);
  float m = -INFINITY;
  v2u vsr, vzr;

  int ahead = 1;
  int late = -SLACK0;
#define THROTTLE(step)                                                                                                   \
  asm volatile("s_cmp_lt_i32 %0, %1\n\ts_cbranch_scc1 1f\n\ts_waitcnt vmcnt(%3)\n\t"                                     \
               "s_cmp_lt_i32 %0, 0\n\ts_cbranch_scc1 1f\n\ts_waitcnt vmcnt(%2)\n1:"                                      \
               ::"s"(ahead), "s"(late), "i"(waitn(CAP1, step)), "i"(waitn(CAP2, step)) : "scc")
#define SCHED(step)                                                                                                      \
  do {                                                                                                                   \
    if ((step) == 0) { due += TAU; late -= SLACK1; }                                                                     \
    if ((step) % 4 == 0) ahead = (int)(due + (step) * TAU / 16 - NOW());                                                 \
  } while (0)

  f32x4 d0, d1;
  v4u kf[4];
  auto frag_read = [&](int half) __attribute__((always_inline)) {
    __builtin_amdgcn_fence(__ATOMIC_ACQ_REL, "wavefront");
#pragma unroll
    for (int kk = 0; kk < 4; ++kk) kf[kk] = *(LAS v4u*)(kbuf + c * 256 + (((4 * kk + g) ^ c) << 4) + 4096 * half);
    __builtin_amdgcn_fence(__ATOMIC_ACQ_REL, "wavefront");
  };
  auto qk_mfma = [&]() __attribute__((always_inline)) {
    f32x4 d = {0.f, 0.f, 0.f, 0.f};
#pragma unroll
    for (int kk = 0; kk < 4; ++kk) d = mfma16(kf[kk], qf[kk], d);
    return d;
  };
  auto put_logits = [&](int it) __attribute__((always_inline)) {
    const unsigned r01 = pk_bf16(u ? d1[0] : d0[0], u ? d1[1] : d0[1]);
    const unsigned r23 = pk_bf16(u ? d1[2] : d0[2], u ? d1[3] : d0[3]);
    v2u sv;
    sv.x = pk_bf16(bf_lo(r01) * SCALE, bf_hi(r01) * SCALE);
    sv.y = pk_bf16(bf_lo(r23) * SCALE, bf_hi(r23) * SCALE);
    sbuf[it * 64] = sv;
    m = fmaxf(fmaxf(m, fmaxf(bf_lo(sv.x), bf_hi(sv.x))), fmaxf(bf_lo(sv.y), bf_hi(sv.y)));
    asm volatile("" ::"v"(m));
  };
  auto k_unit = [&](auto FIRST_, auto LAST_, int j, int it) __attribute__((always_inline)) {
    constexpr bool FIRST = decltype(FIRST_)::value, LAST = decltype(LAST_)::value;
    const int gb = ubase(j);
    const int jn = LAST ? j0 : (j + UPI) & (NUNIT - 1);
    const int gbn = ubase(jn);
    const float kz0 = bf_lo(kzr.x), kz1 = bf_hi(kzr.x), kz2 = bf_lo(kzr.y), kz3 = bf_hi(kzr.y);
    const float ks0 = bf_lo(ksr.x), ks1 = bf_hi(ksr.x), ks2 = bf_lo(ksr.y), ks3 = bf_hi(ksr.y);
    SB();
#pragma unroll
    for (int p = 0; p < 16; ++p) {
      const v4u x = a[p];
      asm volatile("" ::"v"(x));
      if (!FIRST) THROTTLE(p);
      else if (p + DIST >= 16) asm volatile("s_waitcnt vmcnt(%0)" ::"i"(CAP1 - 1 + (p + DIST - CAP1 < 16 ? 2 : 0)));
      SB();
      if (p + DIST < 16) { if (!FIRST) a[p + DIST] = LDK(gb, p + DIST); }
      else if (!LAST) a[(p + DIST) & 15] = LDK(gbn, (p + DIST) & 15);
      else a[(p + DIST) & 15] = LDV(gbn, (p + DIST) & 15);
      if (p == 4) {
        if (LAST) { vsr = ldnt8(rvs, voffT, soff_v(jn)); vzr = ldnt8(rvz, voffT, soff_v(jn)); }
        else { kzr = ldnt8(rkz, voffS, soff_k(jn)); ksr = ldnt8(rks, voffS, soff_k(jn)); }
      }
      SB();
      if (!FIRST) SCHED(p);
      if (!FIRST && p == 0) frag_read(1);
      if (p == 8) frag_read(0);
      const unsigned d01 = pk_bf16((float)(int)x.x - kz0, (float)(int)x.y - kz1);
      const unsigned d23 = pk_bf16((float)(int)x.z - kz2, (float)(int)x.w - kz3);
      v2u kk2;
      kk2.x = pk_bf16(bf_lo(d01) * ks0, bf_hi(d01) * ks1);
      kk2.y = pk_bf16(bf_lo(d23) * ks2, bf_hi(d23) * ks3);
      *(LAS v2u*)(kbuf + (wr0 ^ (32 * (p & 7))) + 512 * p) = kk2;
      if (!FIRST && p == 1) d1 = qk_mfma();
      if (!FIRST && p == 2) put_logits(it - 1);
      if (p == 9) d0 = qk_mfma();
      SB();
    }
  };
  asm volatile("" ::"v"(codew));
  k_unit(TrueT{}, FalseT{}, j0, 0);
#pragma nounroll
  for (int it = 1; it < NIT - 1; ++it) k_unit(FalseT{}, FalseT{}, (j0 + it * UPI) & (NUNIT - 1), it);
  k_unit(FalseT{}, TrueT{}, (j0 + (NIT - 1) * UPI) & (NUNIT - 1), NIT - 1);
  frag_read(1);
  d1 = qk_mfma();
  put_logits(NIT - 1);

  m = fmaxf(m, dpp<ROR8>(m));
  m = xrow_max(m);
  float lsum = 0.f, csum = 0.f;
  f32x4 acc[2][4];
#pragma unroll
  for (int hf = 0; hf < 2; ++hf)
#pragma unroll
    for (int e = 0; e < 4; ++e) acc[hf][e] = f32x4{0.f, 0.f, 0.f, 0.f};
  float wgt[NWAVE], cterm;
  unsigned sideb;
  const int hsh = 16 * (lane & 1);
  const unsigned tagb = ((tag >> hsh) & 0xffu) | (((tag >> hsh) & 0xff00u) << 8);
  LAS float* obuf = (LAS float*)kbuf + c * OSTR + 16 * g;
  auto v_unit = [&](auto LAST_, int j, int it) __attribute__((always_inline)) {
    constexpr bool LAST = decltype(LAST_)::value;
    const int gb = ubase(j);
    const int jn = (j + UPI) & (NUNIT - 1);
    const int gbn = ubase(jn);
    if (LAST) {
#pragma unroll
      for (int n = DIST; n < 16; ++n) a[n] = LDV(gb, n);
    }
    const v2u sv = sbuf[it * 64];
    SB();
    v4u pf, vf[4];
    float p0, p1, p2, p3;
#pragma unroll
    for (int n = 0; n < 16; ++n) {
      if (!LAST) {
        asm volatile("" ::"v"(a[n]));
        THROTTLE(n);
        SB();
        a[(n + DIST) & 15] = LDV(n + DIST < 16 ? gb : gbn, (n + DIST) & 15);
        SB();
        SCHED(n);
      }
      if (n & 1) {
#pragma unroll
        for (int e = 0; e < 4; ++e) vf[e][(n >> 1) & 3] = pk_bf16((float)(int)a[n - 1][e], (float)(int)a[n][e]);
        asm volatile("" ::"v"(vf[0][(n >> 1) & 3]), "v"(vf[1][(n >> 1) & 3]), "v"(vf[2][(n >> 1) & 3]), "v"(vf[3][(n >> 1) & 3]));
      }
      if (n == 0) {
        p0 = __builtin_amdgcn_exp2f((bf_lo(sv.x) - m) * LOG2E); p1 = __builtin_amdgcn_exp2f((bf_hi(sv.x) - m) * LOG2E);
        p2 = __builtin_amdgcn_exp2f((bf_lo(sv.y) - m) * LOG2E); p3 = __builtin_amdgcn_exp2f((bf_hi(sv.y) - m) * LOG2E);
        lsum += (p0 + p1) + (p2 + p3);
        asm volatile("" ::"v"(lsum));
      }
      if (n == (LAST ? 0 : 2)) {
        const unsigned p01 = pk_bf16(p0 * bf_lo(vsr.x), p1 * bf_hi(vsr.x));
        const unsigned p23 = pk_bf16(p2 * bf_lo(vsr.y), p3 * bf_hi(vsr.y));
        csum = fmaf(bf_lo(p01), bf_lo(vzr.x), csum);
        csum = fmaf(bf_hi(p01), bf_hi(vzr.x), csum);
        csum = fmaf(bf_lo(p23), bf_lo(vzr.y), csum);
        csum = fmaf(bf_hi(p23), bf_hi(vzr.y), csum);
        pf.x = u ? 0u : p01;
        pf.y = u ? 0u : p23;
        pf.z = u ? p01 : 0u;
        pf.w = u ? p23 : 0u;
        asm volatile("" ::"v"(pf), "v"(csum));
      }
      if (!LAST && n == 4) {
        vsr = ldnt8(rvs, voffT, soff_v(jn));
        vzr = ldnt8(rvz, voffT, soff_v(jn));
      }
      if (LAST && n == 0) {
        const float lw = xrow_sum(lsum + dpp<ROR8>(lsum)), cw = xrow_sum(csum + dpp<ROR8>(csum));
        if (lane < 8) mbuf[w * 8 + h] = f32x4{m, lw, cw, 0.f};
        __syncthreads();
        f32x4 ml[NWAVE];
#pragma unroll
        for (int i = 0; i < NWAVE; ++i) ml[i] = mbuf[i * 8 + 2 * w + hi];
        const float M = fmaxf(fmaxf(ml[0][0], ml[1][0]), fmaxf(ml[2][0], ml[3][0]));
        float L = 0.f, C = 0.f;
#pragma unroll
        for (int i = 0; i < NWAVE; ++i) {
          wgt[i] = __builtin_amdgcn_exp2f((ml[i][0] - M) * LOG2E);
          L = fmaf(wgt[i], ml[i][1], L);
          C = fmaf(wgt[i], ml[i][2], C);
        }
        const float inv = __builtin_amdgcn_rcpf(L);
#pragma unroll
        for (int i = 0; i < NWAVE; ++i) wgt[i] *= inv;
        cterm = -C * inv;
        const unsigned lh = __float_as_uint(fmaf(M, LOG2E, __builtin_amdgcn_logf(L))) >> hsh;
        sideb = tagb | ((lh & 0xffu) << 8) | ((lh & 0xff00u) << 16);
        asm volatile("" ::"v"(wgt[0]), "v"(wgt[1]), "v"(wgt[2]), "v"(wgt[3]), "v"(cterm), "v"(sideb));
      }
      if (LAST && n == 10) {
#pragma unroll
        for (int e = 0; e < 4; ++e) *(LAS f32x4*)(obuf + 4 * e) = acc[0][e];
      }
      if ((n & 7) == 7) {
#pragma unroll
        for (int e = 0; e < 4; ++e) acc[n >> 3][e] = mfma16(vf[e], pf, acc[n >> 3][e]);
      }
      SB();
    }
  };
#pragma nounroll
  for (int it = 0; it < NIT - 1; ++it) v_unit(FalseT{}, (j0 + it * UPI) & (NUNIT - 1), it);
  v_unit(TrueT{}, (j0 + (NIT - 1) * UPI) & (NUNIT - 1), NIT - 1);
#pragma unroll
  for (int e = 0; e < 4; ++e) *(LAS f32x4*)(obuf + 64 + 4 * e) = acc[1][e];
  __syncthreads();

  {
    f32x4 ob[NWAVE][2];
#pragma unroll
    for (int i = 0; i < NWAVE; ++i) {
      const LAS float* op = (const LAS float*)((LAS unsigned char*)smem + i * WBYTES) + (2 * w + hi) * OSTR + 4 * (lane & 31);
      ob[i][0] = *(const LAS f32x4*)op;
      ob[i][1] = *(const LAS f32x4*)(op + 8 * OSTR);
    }
    f32x4 o = {cterm, cterm, cterm, cterm};
#pragma unroll
    for (int i = 0; i < NWAVE; ++i) o += wgt[i] * (ob[i][0] + ob[i][1]);
    const v4u pk = {(__float_as_uint(o[0]) & 0xffffff00u) | (sideb & 0xffu), (__float_as_uint(o[1]) & 0xffffff00u) | ((sideb >> 8) & 0xffu),
                    (__float_as_uint(o[2]) & 0xffffff00u) | ((sideb >> 16) & 0xffu), (__float_as_uint(o[3]) & 0xffffff00u) | (sideb >> 24)};
    const int cp = 8 * w + (lane >> 3);
    __builtin_amdgcn_raw_buffer_store_b128(pk, rp, (cp * NSPLIT + ((cb + cp) & (NSPLIT - 1))) * 128 + (lane & 7) * 16, 0, AUX_SC1);
  }
  asm volatile("s_waitcnt vmcnt(0)" ::: "memory");
  __builtin_amdgcn_s_sleep(4);
  while ((int)(due + TAU + GATE - NOW()) > 0) __builtin_amdgcn_s_sleep(1);

  {
    int po = ((cb >> 1) * NSPLIT + 16 * w + (lane >> 2)) * 128 + (cb & 1) * 64 + (lane & 3) * 16;
    v4u xo = {0u, 0u, 0u, 0u};
    unsigned spins = 0;
    bool bad = false;
    while (true) {
      asm volatile("" ::: "memory");
      const v4u x = __builtin_amdgcn_raw_buffer_load_b128(rp, po, 0, AUX_SC1);
      unsigned long long okm = __ballot(((x.x & 0xffu) | ((x.z & 0xffu) << 16)) == tagb);
      okm &= okm >> 1;
      okm &= okm >> 2;
      okm = (okm & 0x1111111111111111ull) * 15ull;
      const bool k = (okm >> lane) & 1ull;
      xo = k ? x : xo;
      po = k ? OOB : po;
      if (__all(po == OOB)) break;
      if (++spins > SPIN_CAP) { bad = true; break; }
      __builtin_amdgcn_s_sleep(2);
    }
    const unsigned lhalf = ((xo.y & 0xffu) | ((xo.w & 0xffu) << 8)) << hsh;
    float lse = __uint_as_float(lhalf | (unsigned)__builtin_amdgcn_mov_dpp((int)lhalf, XOR1, 0xf, 0xf, true));
    if (bad) lse = __builtin_nanf("");
    float M = fmaxf(lse, dpp<ROR4>(lse));
    M = fmaxf(M, dpp<ROR8>(M));
    M = xrow_max(M);
    const float e = __builtin_amdgcn_exp2f(lse - M);
    float av[4];
#pragma unroll
    for (int i = 0; i < 4; ++i) av[i] = e * __uint_as_float(xo[i] & 0xffffff00u);
    auto s02 = __builtin_amdgcn_permlane32_swap(__float_as_uint(av[0]), __float_as_uint(av[2]), false, false);
    auto s13 = __builtin_amdgcn_permlane32_swap(__float_as_uint(av[1]), __float_as_uint(av[3]), false, false);
    const float b0 = __uint_as_float(s02[0]) + __uint_as_float(s02[1]);
    const float b1 = __uint_as_float(s13[0]) + __uint_as_float(s13[1]);
    auto sw = __builtin_amdgcn_permlane16_swap(__float_as_uint(b0), __float_as_uint(b1), false, false);
    float t = __uint_as_float(sw[0]) + __uint_as_float(sw[1]);
    t += dpp<ROR4>(t);
    t += dpp<ROR8>(t);
    float L = e + dpp<ROR4>(e);
    L = xrow_sum(L + dpp<ROR8>(L));
    LAS f32x4* fin = (LAS f32x4*)((LAS unsigned char*)smem + NWAVE * WBYTES + NWAVE * 8 * 16);
    if (c < 4) fin[w * 16 + 4 * g + c] = f32x4{t, M, L, 0.f};
    __syncthreads();
    if (tid < 16) {
      f32x4 r[NWAVE];
#pragma unroll
      for (int i = 0; i < NWAVE; ++i) r[i] = fin[i * 16 + tid];
      const float Mx = fmaxf(fmaxf(r[0][1], r[1][1]), fmaxf(r[2][1], r[3][1]));
      float Lx = 0.f, ox = 0.f;
#pragma unroll
      for (int i = 0; i < NWAVE; ++i) {
        const float ee = __builtin_amdgcn_exp2f(r[i][1] - Mx);
        Lx = fmaf(ee, r[i][2], Lx);
        ox = fmaf(ee, r[i][0], ox);
      }
      __builtin_amdgcn_raw_buffer_store_b16((unsigned short)pk_bf16(ox * __builtin_amdgcn_rcpf(Lx), 0.f), RSRC(out, HKV * 8 * DH * 2),
                                            ((hk * 8 + (cb >> 3)) * DH + 16 * (cb & 7) + tid) * 2, 0, AUX_SC1);
      if (cb == 0 && tid == 0) __hip_atomic_store((gu32*)ws + hk * 64, tag, __ATOMIC_RELAXED, __HIP_MEMORY_SCOPE_AGENT);
    }
  }
}
}

extern "C" void kernel_launch(void* const* d_in, const int* in_sizes, int n_in, void* d_out, int out_size, void* d_ws,
                              size_t ws_size, hipStream_t stream) {
  kv_decode<<<HKV * NSPLIT, NTHR, 0, stream>>>((const unsigned short*)d_in[0], (const int*)d_in[1], (const int*)d_in[2],
                                               (const unsigned short*)d_in[3], (const unsigned short*)d_in[4],
                                               (const unsigned short*)d_in[5], (const unsigned short*)d_in[6],
                                               (unsigned char*)d_ws, (unsigned short*)d_out);
}
```

```cpp
#include <hip/hip_runtime.h>
#include <hip/hip_bf16.h>
#include <stdint.h>

namespace {
constexpr int HKV = 4, SKV = 65536, DH = 128, GS = 128, NGRP = SKV / GS;
constexpr int NWAVE = 4, NTHR = NWAVE * 64;
constexpr int NSPLIT = 64;
constexpr int NUNIT = NGRP * 4;
constexpr int UPI = NSPLIT * NWAVE;
constexpr int NIT = NUNIT / UPI;
constexpr int OSTR = 132;
constexpr int WBYTES = 16 * OSTR * 4;
constexpr size_t WS_PAY = 4096;
constexpr unsigned SPIN_CAP = 1u << 20;
constexpr int OOB = 0x40000000;
constexpr int DIST = 8;
constexpr int CAP1 = 5, CAP2 = 6;
constexpr int T_UNIT1 = 350, TAU = 218;
constexpr int SLACK = 50;
constexpr int GATE = 230;
constexpr int CODE_KB = 18;

typedef unsigned v4u __attribute__((ext_vector_type(4)));
typedef unsigned v2u __attribute__((ext_vector_type(2)));
typedef float f32x4 __attribute__((ext_vector_type(4)));
typedef float f32x2 __attribute__((ext_vector_type(2)));
typedef __bf16 bf16x8 __attribute__((ext_vector_type(8)));
typedef __bf16 bf16x2 __attribute__((ext_vector_type(2)));
#define LAS __attribute__((address_space(3)))
typedef __attribute__((address_space(1))) unsigned gu32;
#define RSRC(p, n) __builtin_amdgcn_make_buffer_rsrc((void*)(p), 0, (n), 0x00020000)
constexpr int AUX_NT = 2, AUX_SC1 = 16;
#define SB() __builtin_amdgcn_sched_barrier(0)
#define NOW() ((unsigned)__builtin_amdgcn_s_memrealtime())

__device__ __forceinline__ unsigned pk_bf16(float lo, float hi) {
  f32x2 v = {lo, hi};
  return __builtin_bit_cast(unsigned, __builtin_convertvector(v, bf16x2));
}
__device__ __forceinline__ float bf_lo(unsigned p) { return __uint_as_float(p << 16); }
__device__ __forceinline__ float bf_hi(unsigned p) { return __uint_as_float(p & 0xffff0000u); }
__device__ __forceinline__ f32x4 mfma16(v4u a, v4u b, f32x4 c) {
  return __builtin_amdgcn_mfma_f32_16x16x32_bf16(__builtin_bit_cast(bf16x8, a), __builtin_bit_cast(bf16x8, b), c, 0, 0, 0);
}
__device__ __forceinline__ v4u ldnt(__amdgpu_buffer_rsrc_t rs, int voff, int soff) {
  return __builtin_amdgcn_raw_buffer_load_b128(rs, voff, soff, AUX_NT);
}
__device__ __forceinline__ v2u ldnt8(__amdgpu_buffer_rsrc_t rs, int voff, int soff) {
  return __builtin_amdgcn_raw_buffer_load_b64(rs, voff, soff, AUX_NT);
}
template <int CTRL> __device__ __forceinline__ float dpp(float x) {
  return __int_as_float(__builtin_amdgcn_mov_dpp(__float_as_int(x), CTRL, 0xf, 0xf, true));
}
constexpr int ROR4 = 0x124, ROR8 = 0x128, XOR1 = 0xB1;
__device__ __forceinline__ float xrow_max(float x) {
  auto s = __builtin_amdgcn_permlane16_swap(__float_as_uint(x), __float_as_uint(x), false, false);
  x = fmaxf(__uint_as_float(s[0]), __uint_as_float(s[1]));
  auto t = __builtin_amdgcn_permlane32_swap(__float_as_uint(x), __float_as_uint(x), false, false);
  return fmaxf(__uint_as_float(t[0]), __uint_as_float(t[1]));
}
__device__ __forceinline__ float xrow_sum(float x) {
  auto s = __builtin_amdgcn_permlane16_swap(__float_as_uint(x), __float_as_uint(x), false, false);
  x = __uint_as_float(s[0]) + __uint_as_float(s[1]);
  auto t = __builtin_amdgcn_permlane32_swap(__float_as_uint(x), __float_as_uint(x), false, false);
  return __uint_as_float(t[0]) + __uint_as_float(t[1]);
}
__device__ __forceinline__ int ubase(int j) { return (j >> 1) * 32768 + (j & 1) * 2048; }
__device__ __forceinline__ int soff_k(int j) { return (j >> 2) * 256; }
__device__ __forceinline__ int soff_v(int j) { return (j >> 1) * 128 + (j & 1) * 8; }
constexpr int waitn(int c, int step) { return c - 1 + ((step > 4 && step <= 4 + c) ? 2 : 0); }

struct TrueT { static constexpr bool value = true; };
struct FalseT { static constexpr bool value = false; };
constexpr float LOG2E = 1.4426950408889634f;
constexpr float SCALE = 0.08838834764831845f;

__global__ void __launch_bounds__(NTHR, 2)
kv_decode(const unsigned short* __restrict__ q, const int* __restrict__ qk, const int* __restrict__ qv,
          const unsigned short* __restrict__ ks, const unsigned short* __restrict__ kz,
          const unsigned short* __restrict__ vs, const unsigned short* __restrict__ vz,
          unsigned char* ws, unsigned short* __restrict__ out) {
  __shared__ __attribute__((aligned(16))) unsigned char smem[NWAVE * WBYTES + NWAVE * 8 * 16 + NWAVE * NIT * 64 * 8];
  const int tid = threadIdx.x, lane = tid & 63;
  unsigned long long pc0;
  asm volatile("s_getpc_b64 %0" : "=s"(pc0));
  const unsigned codew = __builtin_amdgcn_raw_buffer_load_b32(
      RSRC(pc0 & ~255ull, CODE_KB * 1024), ((tid & 31) + 32 * ((blockIdx.x >> 3) & 7)) * 128 | (tid & 224) << 22, 0, 0);
  SB();
  asm volatile("" ::"s"(qk), "s"(kz), "s"(vz), "s"(out));
  const int w = __builtin_amdgcn_readfirstlane(tid >> 6);
  const int hk = blockIdx.x & 3, cb = (blockIdx.x >> 3) + 32 * ((blockIdx.x >> 2) & 1);
  const int rot = ((blockIdx.x >> 3) + 2 * w) & 7;
  constexpr int HEADB = SKV * DH * 4;
  const __amdgpu_buffer_rsrc_t rk = RSRC((const char*)qk + (size_t)hk * HEADB, HEADB);
  const int voffK = lane * 16;
#define LDK(gb, p) ldnt(rk, voffK + ((p) & 1) * 1024, (gb) + ((((p) >> 1) ^ rot) << 12))
#define LDV(gb, n) ldnt(rv, (((n) & 4) ? voffV1 : voffV0) + ((n) & 3) * 512 + ((n) >> 3) * 256, (gb))
  const int j0 = (cb * NWAVE + w + 2 * hk * UPI) & (NUNIT - 1);
  v4u a[16];
#pragma unroll
  for (int i = 0; i < 4; ++i) a[i] = LDK(ubase(j0), i);
  SB();
  unsigned due = NOW() + T_UNIT1 - TAU;
  const __amdgpu_buffer_rsrc_t rks = RSRC(ks + (size_t)hk * NGRP * DH, NGRP * DH * 2);
  const __amdgpu_buffer_rsrc_t rkz = RSRC(kz + (size_t)hk * NGRP * DH, NGRP * DH * 2);
  const int voffS = 8 * (lane & 31);
  v2u kzr = ldnt8(rkz, voffS, soff_k(j0));
  v2u ksr = ldnt8(rks, voffS, soff_k(j0));
#pragma unroll
  for (int i = 4; i < 8; ++i) a[i] = LDK(ubase(j0), i);
  SB();
  const int g = lane >> 4, c = lane & 15, u = (lane >> 3) & 1, h = lane & 7, hi = lane >> 5;
  v4u qf[4];
#pragma unroll
  for (int kk = 0; kk < 4; ++kk) qf[kk] = __builtin_nontemporal_load((const v4u*)(q + (size_t)(hk * 8 + h) * DH + 32 * kk + 8 * g));
  SB();
#pragma unroll
  for (int i = 8; i < 16; ++i) a[i] = LDK(ubase(j0), i);
  SB();
  unsigned tag = *(const volatile __attribute__((address_space(4))) unsigned*)(ws + hk * 256) + 0x01010101u;
  tag = tag ? tag : 0x01010101u;
  SB();

  const __amdgpu_buffer_rsrc_t rp = RSRC(ws + WS_PAY + (size_t)hk * NSPLIT * NSPLIT * 64, NSPLIT * NSPLIT * 64);
  const __amdgpu_buffer_rsrc_t rv = RSRC((const char*)qv + (size_t)hk * HEADB, HEADB);
  const __amdgpu_buffer_rsrc_t rvs = RSRC(vs + (size_t)hk * SKV, SKV * 2);
  const __amdgpu_buffer_rsrc_t rvz = RSRC(vz + (size_t)hk * SKV, SKV * 2);
  const int voffV0 = ((g ^ rot) << 12) + c * 16, voffV1 = (((4 + g) ^ rot) << 12) + c * 16;
  const int voffT = 16 * ((4 * u + g) ^ rot);
  LAS unsigned char* kbuf = (LAS unsigned char*)smem + w * WBYTES;
  LAS f32x4* mbuf = (LAS f32x4*)((LAS unsigned char*)smem + NWAVE * WBYTES);
  LAS v2u* sbuf = (LAS v2u*)((LAS unsigned char*)smem + NWAVE * WBYTES + NWAVE * 8 * 16) + w * (NIT * 64) + lane;
  const int wr0 = hi * 256 + ((((lane & 31) >> 1) ^ hi) << 4) + 8 * (lane & 1);
  float m = -INFINITY;
  v2u vsr, vzr;

  int ahead = 1;
#define THROTTLE(step)                                                                                                   \
  asm volatile("s_cmpk_lt_i32 %0, %1\n\ts_cbranch_scc1 1f\n\ts_waitcnt vmcnt(%3)\n\t"                                     \
               "s_cmp_lt_i32 %0, 0\n\ts_cbranch_scc1 1f\n\ts_waitcnt vmcnt(%2)\n1:"                                      \
               ::"s"(ahead), "i"(-SLACK), "i"(waitn(CAP1, step)), "i"(waitn(CAP2, step)) : "scc")
#define SCHED(step)                                                                                                      \
  do {                                                                                                                   \
    if ((step) == 0) due += TAU;                                                                                         \
    if ((step) % 4 == 0) ahead = (int)(due + (step) * TAU / 16 - NOW());                                                 \
  } while (0)

  f32x4 d0, d1;
  v4u kf[4];
  auto frag_read = [&](int half) __attribute__((always_inline)) {
    __builtin_amdgcn_fence(__ATOMIC_ACQ_REL, "wavefront");
#pragma unroll
    for (int kk = 0; kk < 4; ++kk) kf[kk] = *(LAS v4u*)(kbuf + c * 256 + (((4 * kk + g) ^ c) << 4) + 4096 * half);
    __builtin_amdgcn_fence(__ATOMIC_ACQ_REL, "wavefront");
  };
  auto qk_mfma = [&]() __attribute__((always_inline)) {
    f32x4 d = {0.f, 0.f, 0.f, 0.f};
#pragma unroll
    for (int kk = 0; kk < 4; ++kk) d = mfma16(kf[kk], qf[kk], d);
    return d;
  };
  auto put_logits = [&](int it) __attribute__((always_inline)) {
    const unsigned r01 = pk_bf16(u ? d1[0] : d0[0], u ? d1[1] : d0[1]);
    const unsigned r23 = pk_bf16(u ? d1[2] : d0[2], u ? d1[3] : d0[3]);
    v2u sv;
    sv.x = pk_bf16(bf_lo(r01) * SCALE, bf_hi(r01) * SCALE);
    sv.y = pk_bf16(bf_lo(r23) * SCALE, bf_hi(r23) * SCALE);
    sbuf[it * 64] = sv;
    m = fmaxf(fmaxf(m, fmaxf(bf_lo(sv.x), bf_hi(sv.x))), fmaxf(bf_lo(sv.y), bf_hi(sv.y)));
    asm volatile("" ::"v"(m));
  };
  auto k_unit = [&](auto FIRST_, auto LAST_, int j, int it) __attribute__((always_inline)) {
    constexpr bool FIRST = decltype(FIRST_)::value, LAST = decltype(LAST_)::value;
    const int gb = ubase(j);
    const int jn = LAST ? j0 : (j + UPI) & (NUNIT - 1);
    const int gbn = ubase(jn);
    const float kz0 = bf_lo(kzr.x), kz1 = bf_hi(kzr.x), kz2 = bf_lo(kzr.y), kz3 = bf_hi(kzr.y);
    const float ks0 = bf_lo(ksr.x), ks1 = bf_hi(ksr.x), ks2 = bf_lo(ksr.y), ks3 = bf_hi(ksr.y);
    SB();
#pragma unroll
    for (int p = 0; p < 16; ++p) {
      const v4u x = a[p];
      asm volatile("" ::"v"(x));
      if (!FIRST) { if (!LAST || p < 13) THROTTLE(p); }
      else if (p + DIST >= 16) asm volatile("s_waitcnt vmcnt(%0)" ::"i"(CAP1 - 1 + (p + DIST - CAP1 < 16 ? 2 : 0)));
      SB();
      if (p + DIST < 16) { if (!FIRST) a[p + DIST] = LDK(gb, p + DIST); }
      else if (!LAST) a[(p + DIST) & 15] = LDK(gbn, (p + DIST) & 15);
      else a[(p + DIST) & 15] = LDV(gbn, (p + DIST) & 15);
      if (p == 4) {
        if (LAST) { vsr = ldnt8(rvs, voffT, soff_v(jn)); vzr = ldnt8(rvz, voffT, soff_v(jn)); }
        else { kzr = ldnt8(rkz, voffS, soff_k(jn)); ksr = ldnt8(rks, voffS, soff_k(jn)); }
      }
      SB();
      if (!FIRST) SCHED(p);
      if (!FIRST && p == 0) frag_read(1);
      if (p == 8) frag_read(0);
      const unsigned d01 = pk_bf16((float)(int)x.x - kz0, (float)(int)x.y - kz1);
      const unsigned d23 = pk_bf16((float)(int)x.z - kz2, (float)(int)x.w - kz3);
      v2u kk2;
      kk2.x = pk_bf16(bf_lo(d01) * ks0, bf_hi(d01) * ks1);
      kk2.y = pk_bf16(bf_lo(d23) * ks2, bf_hi(d23) * ks3);
      *(LAS v2u*)(kbuf + (wr0 ^ (32 * (p & 7))) + 512 * p) = kk2;
      if (!FIRST && p == 1) d1 = qk_mfma();
      if (!FIRST && p == 2) put_logits(it - 1);
      if (p == 9) d0 = qk_mfma();
      SB();
    }
  };
  asm volatile("" ::"v"(codew));
  k_unit(TrueT{}, FalseT{}, j0, 0);
#pragma nounroll
  for (int it = 1; it < NIT - 1; ++it) k_unit(FalseT{}, FalseT{}, (j0 + it * UPI) & (NUNIT - 1), it);
  k_unit(FalseT{}, TrueT{}, (j0 + (NIT - 1) * UPI) & (NUNIT - 1), NIT - 1);
  frag_read(1);
  d1 = qk_mfma();
  put_logits(NIT - 1);

  m = fmaxf(m, dpp<ROR8>(m));
  m = xrow_max(m);
  float lsum = 0.f, csum = 0.f;
  f32x4 acc[2][4];
#pragma unroll
  for (int hf = 0; hf < 2; ++hf)
#pragma unroll
    for (int e = 0; e < 4; ++e) acc[hf][e] = f32x4{0.f, 0.f, 0.f, 0.f};
  float wgt[NWAVE], cterm;
  unsigned sideb;
  const int hsh = 16 * (lane & 1);
  const unsigned tagb = ((tag >> hsh) & 0xffu) | (((tag >> hsh) & 0xff00u) << 8);
  LAS float* obuf = (LAS float*)kbuf + c * OSTR + 16 * g;
  auto v_unit = [&](auto LAST_, int j, int it) __attribute__((always_inline)) {
    constexpr bool LAST = decltype(LAST_)::value;
    const int gb = ubase(j);
    const int jn = (j + UPI) & (NUNIT - 1);
    const int gbn = ubase(jn);
    if (LAST) {
#pragma unroll
      for (int n = DIST; n < 16; ++n) a[n] = LDV(gb, n);
    }
    const v2u sv = sbuf[it * 64];
    SB();
    v4u pf, vf[4];
    float p0, p1, p2, p3;
#pragma unroll
    for (int n = 0; n < 16; ++n) {
      if (!LAST) {
        asm volatile("" ::"v"(a[n]));
        THROTTLE(n);
        SB();
        a[(n + DIST) & 15] = LDV(n + DIST < 16 ? gb : gbn, (n + DIST) & 15);
        SB();
        SCHED(n);
      }
      if (n & 1) {
#pragma unroll
        for (int e = 0; e < 4; ++e) vf[e][(n >> 1) & 3] = pk_bf16((float)(int)a[n - 1][e], (float)(int)a[n][e]);
        asm volatile("" ::"v"(vf[0][(n >> 1) & 3]), "v"(vf[1][(n >> 1) & 3]), "v"(vf[2][(n >> 1) & 3]), "v"(vf[3][(n >> 1) & 3]));
      }
      if (n == 0) {
        p0 = __builtin_amdgcn_exp2f((bf_lo(sv.x) - m) * LOG2E); p1 = __builtin_amdgcn_exp2f((bf_hi(sv.x) - m) * LOG2E);
        p2 = __builtin_amdgcn_exp2f((bf_lo(sv.y) - m) * LOG2E); p3 = __builtin_amdgcn_exp2f((bf_hi(sv.y) - m) * LOG2E);
        lsum += (p0 + p1) + (p2 + p3);
        asm volatile("" ::"v"(lsum));
      }
      if (n == (LAST ? 0 : 2)) {
        const unsigned p01 = pk_bf16(p0 * bf_lo(vsr.x), p1 * bf_hi(vsr.x));
        const unsigned p23 = pk_bf16(p2 * bf_lo(vsr.y), p3 * bf_hi(vsr.y));
        csum = fmaf(bf_lo(p01), bf_lo(vzr.x), csum);
        csum = fmaf(bf_hi(p01), bf_hi(vzr.x), csum);
        csum = fmaf(bf_lo(p23), bf_lo(vzr.y), csum);
        csum = fmaf(bf_hi(p23), bf_hi(vzr.y), csum);
        pf.x = u ? 0u : p01;
        pf.y = u ? 0u : p23;
        pf.z = u ? p01 : 0u;
        pf.w = u ? p23 : 0u;
        asm volatile("" ::"v"(pf), "v"(csum));
      }
      if (!LAST && n == 4) {
        vsr = ldnt8(rvs, voffT, soff_v(jn));
        vzr = ldnt8(rvz, voffT, soff_v(jn));
      }
      if (LAST && n == 0) {
        const float lw = xrow_sum(lsum + dpp<ROR8>(lsum)), cw = xrow_sum(csum + dpp<ROR8>(csum));
        if (lane < 8) mbuf[w * 8 + h] = f32x4{m, lw, cw, 0.f};
        __syncthreads();
        f32x4 ml[NWAVE];
#pragma unroll
        for (int i = 0; i < NWAVE; ++i) ml[i] = mbuf[i * 8 + 2 * w + hi];
        const float M = fmaxf(fmaxf(ml[0][0], ml[1][0]), fmaxf(ml[2][0], ml[3][0]));
        float L = 0.f, C = 0.f;
#pragma unroll
        for (int i = 0; i < NWAVE; ++i) {
          wgt[i] = __builtin_amdgcn_exp2f((ml[i][0] - M) * LOG2E);
          L = fmaf(wgt[i], ml[i][1], L);
          C = fmaf(wgt[i], ml[i][2], C);
        }
        const float inv = __builtin_amdgcn_rcpf(L);
#pragma unroll
        for (int i = 0; i < NWAVE; ++i) wgt[i] *= inv;
        cterm = -C * inv;
        const unsigned lh = __float_as_uint(fmaf(M, LOG2E, __builtin_amdgcn_logf(L))) >> hsh;
        sideb = tagb | ((lh & 0xffu) << 8) | ((lh & 0xff00u) << 16);
        asm volatile("" ::"v"(wgt[0]), "v"(wgt[1]), "v"(wgt[2]), "v"(wgt[3]), "v"(cterm), "v"(sideb));
      }
      if (LAST && n == 10) {
#pragma unroll
        for (int e = 0; e < 4; ++e) *(LAS f32x4*)(obuf + 4 * e) = acc[0][e];
      }
      if ((n & 7) == 7) {
#pragma unroll
        for (int e = 0; e < 4; ++e) acc[n >> 3][e] = mfma16(vf[e], pf, acc[n >> 3][e]);
      }
      SB();
    }
  };
#pragma nounroll
  for (int it = 0; it < NIT - 1; ++it) v_unit(FalseT{}, (j0 + it * UPI) & (NUNIT - 1), it);
  v_unit(TrueT{}, (j0 + (NIT - 1) * UPI) & (NUNIT - 1), NIT - 1);
#pragma unroll
  for (int e = 0; e < 4; ++e) *(LAS f32x4*)(obuf + 64 + 4 * e) = acc[1][e];
  __syncthreads();

  {
    f32x4 ob[NWAVE][2];
#pragma unroll
    for (int i = 0; i < NWAVE; ++i) {
      const LAS float* op = (const LAS float*)((LAS unsigned char*)smem + i * WBYTES) + (2 * w + hi) * OSTR + 4 * (lane & 31);
      ob[i][0] = *(const LAS f32x4*)op;
      ob[i][1] = *(const LAS f32x4*)(op + 8 * OSTR);
    }
    f32x4 o = {cterm, cterm, cterm, cterm};
#pragma unroll
    for (int i = 0; i < NWAVE; ++i) o += wgt[i] * (ob[i][0] + ob[i][1]);
    const v4u pk = {(__float_as_uint(o[0]) & 0xffffff00u) | (sideb & 0xffu), (__float_as_uint(o[1]) & 0xffffff00u) | ((sideb >> 8) & 0xffu),
                    (__float_as_uint(o[2]) & 0xffffff00u) | ((sideb >> 16) & 0xffu), (__float_as_uint(o[3]) & 0xffffff00u) | (sideb >> 24)};
    const int cp = 8 * w + (lane >> 3);
    __builtin_amdgcn_raw_buffer_store_b128(pk, rp, (cp * NSPLIT + ((cb + cp) & (NSPLIT - 1))) * 128 + (lane & 7) * 16, 0, AUX_SC1);
  }
  asm volatile("s_waitcnt vmcnt(0)" ::: "memory");
  __builtin_amdgcn_s_sleep(4);
  while ((int)(due + TAU + GATE - NOW()) > 0) __builtin_amdgcn_s_sleep(1);

  {
    int po = ((cb >> 1) * NSPLIT + 16 * w + (lane >> 2)) * 128 + (cb & 1) * 64 + (lane & 3) * 16;
    v4u xo = {0u, 0u, 0u, 0u};
    unsigned spins = 0;
    bool bad = false;
    while (true) {
      asm volatile("" ::: "memory");
      const v4u x = __builtin_amdgcn_raw_buffer_load_b128(rp, po, 0, AUX_SC1);
      unsigned long long okm = __ballot(((x.x & 0xffu) | ((x.z & 0xffu) << 16)) == tagb);
      okm &= okm >> 1;
      okm &= okm >> 2;
      okm = (okm & 0x1111111111111111ull) * 15ull;
      const bool k = (okm >> lane) & 1ull;
      xo = k ? x : xo;
      po = k ? OOB : po;
      if (__all(po == OOB)) break;
      if (++spins > SPIN_CAP) { bad = true; break; }
      __builtin_amdgcn_s_sleep(2);
    }
    const unsigned lhalf = ((xo.y & 0xffu) | ((xo.w & 0xffu) << 8)) << hsh;
    float lse = __uint_as_float(lhalf | (unsigned)__builtin_amdgcn_mov_dpp((int)lhalf, XOR1, 0xf, 0xf, true));
    if (bad) lse = __builtin_nanf("");
    float M = fmaxf(lse, dpp<ROR4>(lse));
    M = fmaxf(M, dpp<ROR8>(M));
    M = xrow_max(M);
    const float e = __builtin_amdgcn_exp2f(lse - M);
    float av[4];
#pragma unroll
    for (int i = 0; i < 4; ++i) av[i] = e * __uint_as_float(xo[i] & 0xffffff00u);
    auto s02 = __builtin_amdgcn_permlane32_swap(__float_as_uint(av[0]), __float_as_uint(av[2]), false, false);
    auto s13 = __builtin_amdgcn_permlane32_swap(__float_as_uint(av[1]), __float_as_uint(av[3]), false, false);
    const float b0 = __uint_as_float(s02[0]) + __uint_as_float(s02[1]);
    const float b1 = __uint_as_float(s13[0]) + __uint_as_float(s13[1]);
    auto sw = __builtin_amdgcn_permlane16_swap(__float_as_uint(b0), __float_as_uint(b1), false, false);
    float t = __uint_as_float(sw[0]) + __uint_as_float(sw[1]);
    t += dpp<ROR4>(t);
    t += dpp<ROR8>(t);
    float L = e + dpp<ROR4>(e);
    L = xrow_sum(L + dpp<ROR8>(L));
    LAS f32x4* fin = (LAS f32x4*)((LAS unsigned char*)smem + NWAVE * WBYTES + NWAVE * 8 * 16);
    if (c < 4) fin[w * 16 + 4 * g + c] = f32x4{t, M, L, 0.f};
    __syncthreads();
    if (tid < 16) {
      f32x4 r[NWAVE];
#pragma unroll
      for (int i = 0; i < NWAVE; ++i) r[i] = fin[i * 16 + tid];
      const float Mx = fmaxf(fmaxf(r[0][1], r[1][1]), fmaxf(r[2][1], r[3][1]));
      float Lx = 0.f, ox = 0.f;
#pragma unroll
      for (int i = 0; i < NWAVE; ++i) {
        const float ee = __builtin_amdgcn_exp2f(r[i][1] - Mx);
        Lx = fmaf(ee, r[i][2], Lx);
        ox = fmaf(ee, r[i][0], ox);
      }
      __builtin_amdgcn_raw_buffer_store_b16((unsigned short)pk_bf16(ox * __builtin_amdgcn_rcpf(Lx), 0.f), RSRC(out, HKV * 8 * DH * 2),
                                            ((hk * 8 + (cb >> 3)) * DH + 16 * (cb & 7) + tid) * 2, 0, AUX_SC1);
      if (cb == 0 && tid == 0) __hip_atomic_store((gu32*)ws + hk * 64, tag, __ATOMIC_RELAXED, __HIP_MEMORY_SCOPE_AGENT);
    }
  }
}
}

extern "C" void kernel_launch(void* const* d_in, const int* in_sizes, int n_in, void* d_out, int out_size, void* d_ws,
                              size_t ws_size, hipStream_t stream) {
  kv_decode<<<HKV * NSPLIT, NTHR, 0, stream>>>((const unsigned short*)d_in[0], (const int*)d_in[1], (const int*)d_in[2],
                                               (const unsigned short*)d_in[3], (const unsigned short*)d_in[4],
                                               (const unsigned short*)d_in[5], (const unsigned short*)d_in[6],
                                               (unsigned char*)d_ws, (unsigned short*)d_out);
}
```

```cpp
#include <hip/hip_runtime.h>
#include <hip/hip_bf16.h>
#include <stdint.h>

namespace {
constexpr int HKV = 4, SKV = 65536, DH = 128, GS = 128, NGRP = SKV / GS;
constexpr int NWAVE = 4, NTHR = NWAVE * 64;
constexpr int NSPLIT = 64;
constexpr int NUNIT = NGRP * 4;
constexpr int UPI = NSPLIT * NWAVE;
constexpr int NIT = NUNIT / UPI;
constexpr int OSTR = 132;
constexpr int WBYTES = 16 * OSTR * 4;
constexpr size_t WS_PAY = 4096;
constexpr unsigned SPIN_CAP = 1u << 20;
constexpr int OOB = 0x40000000;
constexpr int DIST = 8;
constexpr int CAP1 = 5, CAP2 = 6;
constexpr int T_UNIT1 = 338, TAU = 218;
constexpr int SLACK0 = 55, SLACK1 = 3;
constexpr int GATE = 230;
constexpr int CODE_KB = 18;

typedef unsigned v4u __attribute__((ext_vector_type(4)));
typedef unsigned v2u __attribute__((ext_vector_type(2)));
typedef float f32x4 __attribute__((ext_vector_type(4)));
typedef float f32x2 __attribute__((ext_vector_type(2)));
typedef __bf16 bf16x8 __attribute__((ext_vector_type(8)));
typedef __bf16 bf16x2 __attribute__((ext_vector_type(2)));
#define LAS __attribute__((address_space(3)))
typedef __attribute__((address_space(1))) unsigned gu32;
#define RSRC(p, n) __builtin_amdgcn_make_buffer_rsrc((void*)(p), 0, (n), 0x00020000)
constexpr int AUX_NT = 2, AUX_SC1 = 16;
#define SB() __builtin_amdgcn_sched_barrier(0)
#define NOW() ((unsigned)__builtin_amdgcn_s_memrealtime())

__device__ __forceinline__ unsigned pk_bf16(float lo, float hi) {
  f32x2 v = {lo, hi};
  return __builtin_bit_cast(unsigned, __builtin_convertvector(v, bf16x2));
}
__device__ __forceinline__ float bf_lo(unsigned p) { return __uint_as_float(p << 16); }
__device__ __forceinline__ float bf_hi(unsigned p) { return __uint_as_float(p & 0xffff0000u); }
__device__ __forceinline__ f32x4 mfma16(v4u a, v4u b, f32x4 c) {
  return __builtin_amdgcn_mfma_f32_16x16x32_bf16(__builtin_bit_cast(bf16x8, a), __builtin_bit_cast(bf16x8, b), c, 0, 0, 0);
}
__device__ __forceinline__ v4u ldnt(__amdgpu_buffer_rsrc_t rs, int voff, int soff) {
  return __builtin_amdgcn_raw_buffer_load_b128(rs, voff, soff, AUX_NT);
}
__device__ __forceinline__ v2u ldnt8(__amdgpu_buffer_rsrc_t rs, int voff, int soff) {
  return __builtin_amdgcn_raw_buffer_load_b64(rs, voff, soff, AUX_NT);
}
template <int CTRL> __device__ __forceinline__ float dpp(float x) {
  return __int_as_float(__builtin_amdgcn_mov_dpp(__float_as_int(x), CTRL, 0xf, 0xf, true));
}
constexpr int ROR4 = 0x124, ROR8 = 0x128, XOR1 = 0xB1;
__device__ __forceinline__ float xrow_max(float x) {
  auto s = __builtin_amdgcn_permlane16_swap(__float_as_uint(x), __float_as_uint(x), false, false);
  x = fmaxf(__uint_as_float(s[0]), __uint_as_float(s[1]));
  auto t = __builtin_amdgcn_permlane32_swap(__float_as_uint(x), __float_as_uint(x), false, false);
  return fmaxf(__uint_as_float(t[0]), __uint_as_float(t[1]));
}
__device__ __forceinline__ float xrow_sum(float x) {
  auto s = __builtin_amdgcn_permlane16_swap(__float_as_uint(x), __float_as_uint(x), false, false);
  x = __uint_as_float(s[0]) + __uint_as_float(s[1]);
  auto t = __builtin_amdgcn_permlane32_swap(__float_as_uint(x), __float_as_uint(x), false, false);
  return __uint_as_float(t[0]) + __uint_as_float(t[1]);
}
__device__ __forceinline__ int ubase(int j) { return (j >> 1) * 32768 + (j & 1) * 2048; }
__device__ __forceinline__ int soff_k(int j) { return (j >> 2) * 256; }
__device__ __forceinline__ int soff_v(int j) { return (j >> 1) * 128 + (j & 1) * 8; }
constexpr int waitn(int c, int step) { return c - 1 + ((step > 4 && step <= 4 + c) ? 2 : 0); }

struct TrueT { static constexpr bool value = true; };
struct FalseT { static constexpr bool value = false; };
constexpr float LOG2E = 1.4426950408889634f;
constexpr float SCALE = 0.08838834764831845f;

__global__ void __launch_bounds__(NTHR, 2)
kv_decode(const unsigned short* __restrict__ q, const int* __restrict__ qk, const int* __restrict__ qv,
          const unsigned short* __restrict__ ks, const unsigned short* __restrict__ kz,
          const unsigned short* __restrict__ vs, const unsigned short* __restrict__ vz,
          unsigned char* ws, unsigned short* __restrict__ out) {
  __shared__ __attribute__((aligned(16))) unsigned char smem[NWAVE * WBYTES + NWAVE * 8 * 16 + NWAVE * NIT * 64 * 8];
  const int tid = threadIdx.x, lane = tid & 63;
  unsigned long long pc0;
  asm volatile("s_getpc_b64 %0" : "=s"(pc0));
  const unsigned codew = __builtin_amdgcn_raw_buffer_load_b32(
      RSRC(pc0 & ~255ull, CODE_KB * 1024), ((tid & 31) + 32 * ((blockIdx.x >> 3) & 7)) * 128 | (tid & 224) << 22, 0, 0);
  SB();
  const int w = __builtin_amdgcn_readfirstlane(tid >> 6);
  const int hk = blockIdx.x & 3, cb = (blockIdx.x >> 3) + 32 * ((blockIdx.x >> 2) & 1);
  const int rot = ((blockIdx.x >> 3) + 2 * w) & 7;
  const int j0 = (cb * NWAVE + w + 2 * hk * UPI) & (NUNIT - 1);
  const int voffK = lane * 16;
  asm volatile("" ::"s"(ubase(j0) + (rot << 12)), "s"(ubase(j0) + ((1 ^ rot) << 12)), "v"(voffK));
  SB();
  asm volatile("" ::"s"(qk), "s"(kz), "s"(vz), "s"(out));
  constexpr int HEADB = SKV * DH * 4;
  const __amdgpu_buffer_rsrc_t rk = RSRC((const char*)qk + (size_t)hk * HEADB, HEADB);
#define LDK(gb, p) ldnt(rk, voffK + ((p) & 1) * 1024, (gb) + ((((p) >> 1) ^ rot) << 12))
#define LDV(gb, n) ldnt(rv, (((n) & 4) ? voffV1 : voffV0) + ((n) & 3) * 512 + ((n) >> 3) * 256, (gb))
  v4u a[16];
#pragma unroll
  for (int i = 0; i < 4; ++i) a[i] = LDK(ubase(j0), i);
  SB();
  const unsigned long long tbeg = __builtin_amdgcn_s_memrealtime();
  const __amdgpu_buffer_rsrc_t rks = RSRC(ks + (size_t)hk * NGRP * DH, NGRP * DH * 2);
  const __amdgpu_buffer_rsrc_t rkz = RSRC(kz + (size_t)hk * NGRP * DH, NGRP * DH * 2);
  const int voffS = 8 * (lane & 31);
  v2u kzr = ldnt8(rkz, voffS, soff_k(j0));
  v2u ksr = ldnt8(rks, voffS, soff_k(j0));
#pragma unroll
  for (int i = 4; i < 8; ++i) a[i] = LDK(ubase(j0), i);
  SB();
  const int g = lane >> 4, c = lane & 15, u = (lane >> 3) & 1, h = lane & 7, hi = lane >> 5;
  v4u qf[4];
#pragma unroll
  for (int kk = 0; kk < 4; ++kk) qf[kk] = __builtin_nontemporal_load((const v4u*)(q + (size_t)(hk * 8 + h) * DH + 32 * kk + 8 * g));
  SB();
#pragma unroll
  for (int i = 8; i < 16; ++i) a[i] = LDK(ubase(j0), i);
  SB();
  asm volatile("" ::"s"(tbeg));
  unsigned due = (unsigned)tbeg + T_UNIT1 - TAU;
  unsigned tag = *(const volatile __attribute__((address_space(4))) unsigned*)(ws + hk * 256) + 0x01010101u;
  tag = tag ? tag : 0x01010101u;
  SB();

  const __amdgpu_buffer_rsrc_t rp = RSRC(ws + WS_PAY + (size_t)hk * NSPLIT * NSPLIT * 64, NSPLIT * NSPLIT * 64);
  const __amdgpu_buffer_rsrc_t rv = RSRC((const char*)qv + (size_t)hk * HEADB, HEADB);
  const __amdgpu_buffer_rsrc_t rvs = RSRC(vs + (size_t)hk * SKV, SKV * 2);
  const __amdgpu_buffer_rsrc_t rvz = RSRC(vz + (size_t)hk * SKV, SKV * 2);
  const int voffV0 = ((g ^ rot) << 12) + c * 16, voffV1 = (((4 + g) ^ rot) << 12) + c * 16;
  const int voffT = 16 * ((4 * u + g) ^ rot);
  LAS unsigned char* kbuf = (LAS unsigned char*)smem + w * WBYTES;
  LAS f32x4* mbuf = (LAS f32x4*)((LAS unsigned char*)smem + NWAVE * WBYTES);
  LAS v2u* sbuf = (LAS v2u*)((LAS unsigned char*)smem + NWAVE * WBYTES + NWAVE * 8 * 16) + w * (NIT * 64) + lane;
  const int wr0 = hi * 256 + ((((lane & 31) >> 1) ^ hi) << 4) + 8 * (lane & 1);
  float m = -INFINITY;
  v2u vsr, vzr;

  int ahead = 1;
  int late = -SLACK0;
#define THROTTLE(step)                                                                                                   \
  asm volatile("s_cmp_lt_i32 %0, %1\n\ts_cbranch_scc1 1f\n\ts_waitcnt vmcnt(%3)\n\t"                                     \
               "s_cmp_lt_i32 %0, 0\n\ts_cbranch_scc1 1f\n\ts_waitcnt vmcnt(%2)\n1:"                                      \
               ::"s"(ahead), "s"(late), "i"(waitn(CAP1, step)), "i"(waitn(CAP2, step)) : "scc")
#define SCHED(step)                                                                                                      \
  do {                                                                                                                   \
    if ((step) == 0) { due += TAU; late -= SLACK1; }                                                                     \
    if ((step) % 4 == 0) ahead = (int)(due + (step) * TAU / 16 - NOW());                                                 \
  } while (0)

  f32x4 d0, d1;
  v4u kf[4];
  auto frag_read = [&](int half) __attribute__((always_inline)) {
    __builtin_amdgcn_fence(__ATOMIC_ACQ_REL, "wavefront");
#pragma unroll
    for (int kk = 0; kk < 4; ++kk) kf[kk] = *(LAS v4u*)(kbuf + c * 256 + (((4 * kk + g) ^ c) << 4) + 4096 * half);
    __builtin_amdgcn_fence(__ATOMIC_ACQ_REL, "wavefront");
  };
  auto qk_mfma = [&]() __attribute__((always_inline)) {
    f32x4 d = {0.f, 0.f, 0.f, 0.f};
#pragma unroll
    for (int kk = 0; kk < 4; ++kk) d = mfma16(kf[kk], qf[kk], d);
    return d;
  };
  auto put_logits = [&](int it) __attribute__((always_inline)) {
    const unsigned r01 = pk_bf16(u ? d1[0] : d0[0], u ? d1[1] : d0[1]);
    const unsigned r23 = pk_bf16(u ? d1[2] : d0[2], u ? d1[3] : d0[3]);
    v2u sv;
    sv.x = pk_bf16(bf_lo(r01) * SCALE, bf_hi(r01) * SCALE);
    sv.y = pk_bf16(bf_lo(r23) * SCALE, bf_hi(r23) * SCALE);
    sbuf[it * 64] = sv;
    m = fmaxf(fmaxf(m, fmaxf(bf_lo(sv.x), bf_hi(sv.x))), fmaxf(bf_lo(sv.y), bf_hi(sv.y)));
    asm volatile("" ::"v"(m));
  };
  auto k_unit = [&](auto FIRST_, auto LAST_, int j, int it) __attribute__((always_inline)) {
    constexpr bool FIRST = decltype(FIRST_)::value, LAST = decltype(LAST_)::value;
    const int gb = ubase(j);
    const int jn = LAST ? j0 : (j + UPI) & (NUNIT - 1);
    const int gbn = ubase(jn);
    const float kz0 = bf_lo(kzr.x), kz1 = bf_hi(kzr.x), kz2 = bf_lo(kzr.y), kz3 = bf_hi(kzr.y);
    const float ks0 = bf_lo(ksr.x), ks1 = bf_hi(ksr.x), ks2 = bf_lo(ksr.y), ks3 = bf_hi(ksr.y);
    SB();
#pragma unroll
    for (int p = 0; p < 16; ++p) {
      const v4u x = a[p];
      asm volatile("" ::"v"(x));
      if (!FIRST) { if (!LAST || p < 13) THROTTLE(p); }
      else if (p + DIST >= 16) asm volatile("s_waitcnt vmcnt(%0)" ::"i"(CAP1 - 1 + (p + DIST - CAP1 < 16 ? 2 : 0)));
      SB();
      if (p + DIST < 16) { if (!FIRST) a[p + DIST] = LDK(gb, p + DIST); }
      else if (!LAST) a[(p + DIST) & 15] = LDK(gbn, (p + DIST) & 15);
      else a[(p + DIST) & 15] = LDV(gbn, (p + DIST) & 15);
      if (p == 4) {
        if (LAST) { vsr = ldnt8(rvs, voffT, soff_v(jn)); vzr = ldnt8(rvz, voffT, soff_v(jn)); }
        else { kzr = ldnt8(rkz, voffS, soff_k(jn)); ksr = ldnt8(rks, voffS, soff_k(jn)); }
      }
      SB();
      if (!FIRST) SCHED(p);
      if (!FIRST && p == 0) frag_read(1);
      if (p == 8) frag_read(0);
      const unsigned d01 = pk_bf16((float)(int)x.x - kz0, (float)(int)x.y - kz1);
      const unsigned d23 = pk_bf16((float)(int)x.z - kz2, (float)(int)x.w - kz3);
      v2u kk2;
      kk2.x = pk_bf16(bf_lo(d01) * ks0, bf_hi(d01) * ks1);
      kk2.y = pk_bf16(bf_lo(d23) * ks2, bf_hi(d23) * ks3);
      *(LAS v2u*)(kbuf + (wr0 ^ (32 * (p & 7))) + 512 * p) = kk2;
      if (!FIRST && p == 1) d1 = qk_mfma();
      if (!FIRST && p == 2) put_logits(it - 1);
      if (p == 9) d0 = qk_mfma();
      SB();
    }
  };
  asm volatile("" ::"v"(codew));
  k_unit(TrueT{}, FalseT{}, j0, 0);
#pragma nounroll
  for (int it = 1; it < NIT - 1; ++it) k_unit(FalseT{}, FalseT{}, (j0 + it * UPI) & (NUNIT - 1), it);
  k_unit(FalseT{}, TrueT{}, (j0 + (NIT - 1) * UPI) & (NUNIT - 1), NIT - 1);
  frag_read(1);
  d1 = qk_mfma();
  put_logits(NIT - 1);

  m = fmaxf(m, dpp<ROR8>(m));
  m = xrow_max(m);
  float lsum = 0.f, csum = 0.f;
  f32x4 acc[2][4];
#pragma unroll
  for (int hf = 0; hf < 2; ++hf)
#pragma unroll
    for (int e = 0; e < 4; ++e) acc[hf][e] = f32x4{0.f, 0.f, 0.f, 0.f};
  float wgt[NWAVE], cterm;
  unsigned sideb;
  const int hsh = 16 * (lane & 1);
  const unsigned tagb = ((tag >> hsh) & 0xffu) | (((tag >> hsh) & 0xff00u) << 8);
  LAS float* obuf = (LAS float*)kbuf + c * OSTR + 16 * g;
  auto v_unit = [&](auto LAST_, int j, int it) __attribute__((always_inline)) {
    constexpr bool LAST = decltype(LAST_)::value;
    const int gb = ubase(j);
    const int jn = (j + UPI) & (NUNIT - 1);
    const int gbn = ubase(jn);
    if (LAST) {
#pragma unroll
      for (int n = DIST; n < 16; ++n) a[n] = LDV(gb, n);
    }
    const v2u sv = sbuf[it * 64];
    SB();
    v4u pf, vf[4];
    float p0, p1, p2, p3;
#pragma unroll
    for (int n = 0; n < 16; ++n) {
      if (!LAST) {
        asm volatile("" ::"v"(a[n]));
        THROTTLE(n);
        SB();
        a[(n + DIST) & 15] = LDV(n + DIST < 16 ? gb : gbn, (n + DIST) & 15);
        SB();
        SCHED(n);
      }
      if (n & 1) {
#pragma unroll
        for (int e = 0; e < 4; ++e) vf[e][(n >> 1) & 3] = pk_bf16((float)(int)a[n - 1][e], (float)(int)a[n][e]);
        asm volatile("" ::"v"(vf[0][(n >> 1) & 3]), "v"(vf[1][(n >> 1) & 3]), "v"(vf[2][(n >> 1) & 3]), "v"(vf[3][(n >> 1) & 3]));
      }
      if (n == 0) {
        p0 = __builtin_amdgcn_exp2f((bf_lo(sv.x) - m) * LOG2E); p1 = __builtin_amdgcn_exp2f((bf_hi(sv.x) - m) * LOG2E);
        p2 = __builtin_amdgcn_exp2f((bf_lo(sv.y) - m) * LOG2E); p3 = __builtin_amdgcn_exp2f((bf_hi(sv.y) - m) * LOG2E);
        lsum += (p0 + p1) + (p2 + p3);
        asm volatile("" ::"v"(lsum));
      }
      if (n == (LAST ? 0 : 2)) {
        const unsigned p01 = pk_bf16(p0 * bf_lo(vsr.x), p1 * bf_hi(vsr.x));
        const unsigned p23 = pk_bf16(p2 * bf_lo(vsr.y), p3 * bf_hi(vsr.y));
        csum = fmaf(bf_lo(p01), bf_lo(vzr.x), csum);
        csum = fmaf(bf_hi(p01), bf_hi(vzr.x), csum);
        csum = fmaf(bf_lo(p23), bf_lo(vzr.y), csum);
        csum = fmaf(bf_hi(p23), bf_hi(vzr.y), csum);
        pf.x = u ? 0u : p01;
        pf.y = u ? 0u : p23;
        pf.z = u ? p01 : 0u;
        pf.w = u ? p23 : 0u;
        asm volatile("" ::"v"(pf), "v"(csum));
      }
      if (!LAST && n == 4) {
        vsr = ldnt8(rvs, voffT, soff_v(jn));
        vzr = ldnt8(rvz, voffT, soff_v(jn));
      }
      if (LAST && n == 0) {
        const float lw = xrow_sum(lsum + dpp<ROR8>(lsum)), cw = xrow_sum(csum + dpp<ROR8>(csum));
        if (lane < 8) mbuf[w * 8 + h] = f32x4{m, lw, cw, 0.f};
        __syncthreads();
        f32x4 ml[NWAVE];
#pragma unroll
        for (int i = 0; i < NWAVE; ++i) ml[i] = mbuf[i * 8 + 2 * w + hi];
        const float M = fmaxf(fmaxf(ml[0][0], ml[1][0]), fmaxf(ml[2][0], ml[3][0]));
        float L = 0.f, C = 0.f;
#pragma unroll
        for (int i = 0; i < NWAVE; ++i) {
          wgt[i] = __builtin_amdgcn_exp2f((ml[i][0] - M) * LOG2E);
          L = fmaf(wgt[i], ml[i][1], L);
          C = fmaf(wgt[i], ml[i][2], C);
        }
        const float inv = __builtin_amdgcn_rcpf(L);
#pragma unroll
        for (int i = 0; i < NWAVE; ++i) wgt[i] *= inv;
        cterm = -C * inv;
        const unsigned lh = __float_as_uint(fmaf(M, LOG2E, __builtin_amdgcn_logf(L))) >> hsh;
        sideb = tagb | ((lh & 0xffu) << 8) | ((lh & 0xff00u) << 16);
        asm volatile("" ::"v"(wgt[0]), "v"(wgt[1]), "v"(wgt[2]), "v"(wgt[3]), "v"(cterm), "v"(sideb));
      }
      if (LAST && n == 10) {
#pragma unroll
        for (int e = 0; e < 4; ++e) *(LAS f32x4*)(obuf + 4 * e) = acc[0][e];
      }
      if ((n & 7) == 7) {
#pragma unroll
        for (int e = 0; e < 4; ++e) acc[n >> 3][e] = mfma16(vf[e], pf, acc[n >> 3][e]);
      }
      SB();
    }
  };
#pragma nounroll
  for (int it = 0; it < NIT - 1; ++it) v_unit(FalseT{}, (j0 + it * UPI) & (NUNIT - 1), it);
  v_unit(TrueT{}, (j0 + (NIT - 1) * UPI) & (NUNIT - 1), NIT - 1);
#pragma unroll
  for (int e = 0; e < 4; ++e) *(LAS f32x4*)(obuf + 64 + 4 * e) = acc[1][e];
  __syncthreads();

  {
    f32x4 ob[NWAVE][2];
#pragma unroll
    for (int i = 0; i < NWAVE; ++i) {
      const LAS float* op = (const LAS float*)((LAS unsigned char*)smem + i * WBYTES) + (2 * w + hi) * OSTR + 4 * (lane & 31);
      ob[i][0] = *(const LAS f32x4*)op;
      ob[i][1] = *(const LAS f32x4*)(op + 8 * OSTR);
    }
    f32x4 o = {cterm, cterm, cterm, cterm};
#pragma unroll
    for (int i = 0; i < NWAVE; ++i) o += wgt[i] * (ob[i][0] + ob[i][1]);
    const v4u pk = {(__float_as_uint(o[0]) & 0xffffff00u) | (sideb & 0xffu), (__float_as_uint(o[1]) & 0xffffff00u) | ((sideb >> 8) & 0xffu),
                    (__float_as_uint(o[2]) & 0xffffff00u) | ((sideb >> 16) & 0xffu), (__float_as_uint(o[3]) & 0xffffff00u) | (sideb >> 24)};
    const int cp = 8 * w + (lane >> 3);
    __builtin_amdgcn_raw_buffer_store_b128(pk, rp, (cp * NSPLIT + ((cb + cp) & (NSPLIT - 1))) * 128 + (lane & 7) * 16, 0, AUX_SC1);
  }
  asm volatile("s_waitcnt vmcnt(0)" ::: "memory");
  __builtin_amdgcn_s_sleep(4);
  while ((int)(due + TAU + GATE - NOW()) > 0) __builtin_amdgcn_s_sleep(1);

  {
    int po = ((cb >> 1) * NSPLIT + 16 * w + (lane >> 2)) * 128 + (cb & 1) * 64 + (lane & 3) * 16;
    v4u xo = {0u, 0u, 0u, 0u};
    unsigned spins = 0;
    bool bad = false;
    while (true) {
      asm volatile("" ::: "memory");
      const v4u x = __builtin_amdgcn_raw_buffer_load_b128(rp, po, 0, AUX_SC1);
      unsigned long long okm = __ballot(((x.x & 0xffu) | ((x.z & 0xffu) << 16)) == tagb);
      okm &= okm >> 1;
      okm &= okm >> 2;
      okm = (okm & 0x1111111111111111ull) * 15ull;
      const bool k = (okm >> lane) & 1ull;
      xo = k ? x : xo;
      po = k ? OOB : po;
      if (__all(po == OOB)) break;
      if (++spins > SPIN_CAP) { bad = true; break; }
      __builtin_amdgcn_s_sleep(2);
    }
    const unsigned lhalf = ((xo.y & 0xffu) | ((xo.w & 0xffu) << 8)) << hsh;
    float lse = __uint_as_float(lhalf | (unsigned)__builtin_amdgcn_mov_dpp((int)lhalf, XOR1, 0xf, 0xf, true));
    if (bad) lse = __builtin_nanf("");
    float M = fmaxf(lse, dpp<ROR4>(lse));
    M = fmaxf(M, dpp<ROR8>(M));
    M = xrow_max(M);
    const float e = __builtin_amdgcn_exp2f(lse - M);
    float av[4];
#pragma unroll
    for (int i = 0; i < 4; ++i) av[i] = e * __uint_as_float(xo[i] & 0xffffff00u);
    auto s02 = __builtin_amdgcn_permlane32_swap(__float_as_uint(av[0]), __float_as_uint(av[2]), false, false);
    auto s13 = __builtin_amdgcn_permlane32_swap(__float_as_uint(av[1]), __float_as_uint(av[3]), false, false);
    const float b0 = __uint_as_float(s02[0]) + __uint_as_float(s02[1]);
    const float b1 = __uint_as_float(s13[0]) + __uint_as_float(s13[1]);
    auto sw = __builtin_amdgcn_permlane16_swap(__float_as_uint(b0), __float_as_uint(b1), false, false);
    float t = __uint_as_float(sw[0]) + __uint_as_float(sw[1]);
    t += dpp<ROR4>(t);
    t += dpp<ROR8>(t);
    float L = e + dpp<ROR4>(e);
    L = xrow_sum(L + dpp<ROR8>(L));
    LAS f32x4* fin = (LAS f32x4*)((LAS unsigned char*)smem + NWAVE * WBYTES + NWAVE * 8 * 16);
    if (c < 4) fin[w * 16 + 4 * g + c] = f32x4{t, M, L, 0.f};
    __syncthreads();
    if (tid < 16) {
      f32x4 r[NWAVE];
#pragma unroll
      for (int i = 0; i < NWAVE; ++i) r[i] = fin[i * 16 + tid];
      const float Mx = fmaxf(fmaxf(r[0][1], r[1][1]), fmaxf(r[2][1], r[3][1]));
      float Lx = 0.f, ox = 0.f;
#pragma unroll
      for (int i = 0; i < NWAVE; ++i) {
        const float ee = __builtin_amdgcn_exp2f(r[i][1] - Mx);
        Lx = fmaf(ee, r[i][2], Lx);
        ox = fmaf(ee, r[i][0], ox);
      }
      __builtin_amdgcn_raw_buffer_store_b16((unsigned short)pk_bf16(ox * __builtin_amdgcn_rcpf(Lx), 0.f), RSRC(out, HKV * 8 * DH * 2),
                                            ((hk * 8 + (cb >> 3)) * DH + 16 * (cb & 7) + tid) * 2, 0, AUX_SC1);
      if (cb == 0 && tid == 0) __hip_atomic_store((gu32*)ws + hk * 64, tag, __ATOMIC_RELAXED, __HIP_MEMORY_SCOPE_AGENT);
    }
  }
}
}

extern "C" void kernel_launch(void* const* d_in, const int* in_sizes, int n_in, void* d_out, int out_size, void* d_ws,
                              size_t ws_size, hipStream_t stream) {
  kv_decode<<<HKV * NSPLIT, NTHR, 0, stream>>>((const unsigned short*)d_in[0], (const int*)d_in[1], (const int*)d_in[2],
                                               (const unsigned short*)d_in[3], (const unsigned short*)d_in[4],
                                               (const unsigned short*)d_in[5], (const unsigned short*)d_in[6],
                                               (unsigned char*)d_ws, (unsigned short*)d_out);
}
```

```cpp
#include <hip/hip_runtime.h>
#include <hip/hip_bf16.h>
#include <stdint.h>

namespace {
constexpr int HKV = 4, SKV = 65536, DH = 128, GS = 128, NGRP = SKV / GS;
constexpr int NWAVE = 4, NTHR = NWAVE * 64;
constexpr int NSPLIT = 64;
constexpr int NUNIT = NGRP * 4;
constexpr int UPI = NSPLIT * NWAVE;
constexpr int NIT = NUNIT / UPI;
constexpr int OSTR = 132;
constexpr int WBYTES = 16 * OSTR * 4;
constexpr size_t WS_PAY = 4096;
constexpr unsigned SPIN_CAP = 1u << 20;
constexpr int OOB = 0x40000000;
constexpr int DIST = 8;
constexpr int CAP1 = 5, CAP2 = 6;
constexpr int T_UNIT1 = 350, TAU = 218;
constexpr int SLACK0 = 50, SLACK1 = 3;
constexpr int GATE = 210;
constexpr int LASTP = 5;
constexpr int CODE_BYTES = 19200;

typedef unsigned v4u __attribute__((ext_vector_type(4)));
typedef unsigned v2u __attribute__((ext_vector_type(2)));
typedef float f32x4 __attribute__((ext_vector_type(4)));
typedef float f32x2 __attribute__((ext_vector_type(2)));
typedef __bf16 bf16x8 __attribute__((ext_vector_type(8)));
typedef __bf16 bf16x2 __attribute__((ext_vector_type(2)));
#define LAS __attribute__((address_space(3)))
typedef __attribute__((address_space(1))) unsigned gu32;
#define RSRC(p, n) __builtin_amdgcn_make_buffer_rsrc((void*)(p), 0, (n), 0x00020000)
constexpr int AUX_NT = 2, AUX_SC1 = 16;
#define SB() __builtin_amdgcn_sched_barrier(0)
#define NOW() ((unsigned)__builtin_amdgcn_s_memrealtime())

__device__ __forceinline__ unsigned pk_bf16(float lo, float hi) {
  f32x2 v = {lo, hi};
  return __builtin_bit_cast(unsigned, __builtin_convertvector(v, bf16x2));
}
__device__ __forceinline__ float bf_lo(unsigned p) { return __uint_as_float(p << 16); }
__device__ __forceinline__ float bf_hi(unsigned p) { return __uint_as_float(p & 0xffff0000u); }
__device__ __forceinline__ f32x4 mfma16(v4u a, v4u b, f32x4 c) {
  return __builtin_amdgcn_mfma_f32_16x16x32_bf16(__builtin_bit_cast(bf16x8, a), __builtin_bit_cast(bf16x8, b), c, 0, 0, 0);
}
__device__ __forceinline__ v4u ldnt(__amdgpu_buffer_rsrc_t rs, int voff, int soff) {
  return __builtin_amdgcn_raw_buffer_load_b128(rs, voff, soff, AUX_NT);
}
__device__ __forceinline__ v2u ldnt8(__amdgpu_buffer_rsrc_t rs, int voff, int soff) {
  return __builtin_amdgcn_raw_buffer_load_b64(rs, voff, soff, AUX_NT);
}
template <int CTRL> __device__ __forceinline__ float dpp(float x) {
  return __int_as_float(__builtin_amdgcn_mov_dpp(__float_as_int(x), CTRL, 0xf, 0xf, true));
}
constexpr int ROR4 = 0x124, ROR8 = 0x128, XOR1 = 0xB1;
__device__ __forceinline__ float xrow_max(float x) {
  auto s = __builtin_amdgcn_permlane16_swap(__float_as_uint(x), __float_as_uint(x), false, false);
  x = fmaxf(__uint_as_float(s[0]), __uint_as_float(s[1]));
  auto t = __builtin_amdgcn_permlane32_swap(__float_as_uint(x), __float_as_uint(x), false, false);
  return fmaxf(__uint_as_float(t[0]), __uint_as_float(t[1]));
}
__device__ __forceinline__ float xrow_sum(float x) {
  auto s = __builtin_amdgcn_permlane16_swap(__float_as_uint(x), __float_as_uint(x), false, false);
  x = __uint_as_float(s[0]) + __uint_as_float(s[1]);
  auto t = __builtin_amdgcn_permlane32_swap(__float_as_uint(x), __float_as_uint(x), false, false);
  return __uint_as_float(t[0]) + __uint_as_float(t[1]);
}
__device__ __forceinline__ int ubase(int j) { return (j >> 1) * 32768 + (j & 1) * 2048; }
__device__ __forceinline__ int soff_k(int j) { return (j >> 2) * 256; }
__device__ __forceinline__ int soff_v(int j) { return (j >> 1) * 128 + (j & 1) * 8; }
constexpr int waitn(int c, int step) { return c - 1 + ((step > 4 && step <= 4 + c) ? 2 : 0); }

struct TrueT { static constexpr bool value = true; };
struct FalseT { static constexpr bool value = false; };
constexpr float LOG2E = 1.4426950408889634f;
constexpr float SCALE = 0.08838834764831845f;

__global__ void __launch_bounds__(NTHR, 2)
kv_decode(const unsigned short* __restrict__ q, const int* __restrict__ qk, const int* __restrict__ qv,
          const unsigned short* __restrict__ ks, const unsigned short* __restrict__ kz,
          const unsigned short* __restrict__ vs, const unsigned short* __restrict__ vz,
          unsigned char* ws, unsigned short* __restrict__ out) {
  __shared__ __attribute__((aligned(16))) unsigned char smem[NWAVE * WBYTES + NWAVE * 8 * 16 + NWAVE * NIT * 64 * 8];
  const int tid = threadIdx.x, lane = tid & 63;
  unsigned long long pc0;
  asm volatile("s_getpc_b64 %0" : "=s"(pc0));
  const unsigned codew = __builtin_amdgcn_raw_buffer_load_b32(
      RSRC(pc0 & ~255ull, CODE_BYTES), ((tid & 31) + 32 * ((blockIdx.x >> 3) & 7)) * 128 | (tid & 224) << 22, 0, 0);
  SB();
  const int w = __builtin_amdgcn_readfirstlane(tid >> 6);
  const int hk = blockIdx.x & 3, cb = (blockIdx.x >> 3) + 32 * ((blockIdx.x >> 2) & 1);
  const int rot = ((blockIdx.x >> 3) + 2 * w) & 7;
  const int j0 = (cb * NWAVE + w + 2 * hk * UPI) & (NUNIT - 1);
  const int voffK = lane * 16;
  asm volatile("" ::"s"(ubase(j0) + (rot << 12)), "s"(ubase(j0) + ((1 ^ rot) << 12)), "v"(voffK));
  SB();
  asm volatile("" ::"s"(qk), "s"(kz), "s"(vz), "s"(out));
  constexpr int HEADB = SKV * DH * 4;
  const __amdgpu_buffer_rsrc_t rk = RSRC((const char*)qk + (size_t)hk * HEADB, HEADB);
#define LDK(gb, p) ldnt(rk, voffK + ((p) & 1) * 1024, (gb) + ((((p) >> 1) ^ rot) << 12))
#define LDV(gb, n) ldnt(rv, (((n) & 4) ? voffV1 : voffV0) + ((n) & 3) * 512 + ((n) >> 3) * 256, (gb))
  v4u a[16];
#pragma unroll
  for (int i = 0; i < 4; ++i) a[i] = LDK(ubase(j0), i);
  SB();
  const unsigned long long tbeg = __builtin_amdgcn_s_memrealtime();
  const __amdgpu_buffer_rsrc_t rks = RSRC(ks + (size_t)hk * NGRP * DH, NGRP * DH * 2);
  const __amdgpu_buffer_rsrc_t rkz = RSRC(kz + (size_t)hk * NGRP * DH, NGRP * DH * 2);
  const int voffS = 8 * (lane & 31);
  v2u kzr = ldnt8(rkz, voffS, soff_k(j0));
  v2u ksr = ldnt8(rks, voffS, soff_k(j0));
#pragma unroll
  for (int i = 4; i < 8; ++i) a[i] = LDK(ubase(j0), i);
  SB();
  const int g = lane >> 4, c = lane & 15, u = (lane >> 3) & 1, h = lane & 7, hi = lane >> 5;
  v4u qf[4];
#pragma unroll
  for (int kk = 0; kk < 4; ++kk) qf[kk] = __builtin_nontemporal_load((const v4u*)(q + (size_t)(hk * 8 + h) * DH + 32 * kk + 8 * g));
  SB();
#pragma unroll
  for (int i = 8; i < 16; ++i) a[i] = LDK(ubase(j0), i);
  SB();
  asm volatile("" ::"s"(tbeg));
  unsigned due = (unsigned)tbeg + T_UNIT1 - TAU;
  unsigned tag = *(const volatile __attribute__((address_space(4))) unsigned*)(ws + hk * 256) + 0x01010101u;
  tag = tag ? tag : 0x01010101u;
  SB();

  const __amdgpu_buffer_rsrc_t rp = RSRC(ws + WS_PAY + (size_t)hk * NSPLIT * NSPLIT * 64, NSPLIT * NSPLIT * 64);
  const __amdgpu_buffer_rsrc_t rv = RSRC((const char*)qv + (size_t)hk * HEADB, HEADB);
  const __amdgpu_buffer_rsrc_t rvs = RSRC(vs + (size_t)hk * SKV, SKV * 2);
  const __amdgpu_buffer_rsrc_t rvz = RSRC(vz + (size_t)hk * SKV, SKV * 2);
  const int voffV0 = ((g ^ rot) << 12) + c * 16, voffV1 = (((4 + g) ^ rot) << 12) + c * 16;
  const int voffT = 16 * ((4 * u + g) ^ rot);
  LAS unsigned char* kbuf = (LAS unsigned char*)smem + w * WBYTES;
  LAS f32x4* mbuf = (LAS f32x4*)((LAS unsigned char*)smem + NWAVE * WBYTES);
  LAS v2u* sbuf = (LAS v2u*)((LAS unsigned char*)smem + NWAVE * WBYTES + NWAVE * 8 * 16) + w * (NIT * 64) + lane;
  const int wr0 = hi * 256 + ((((lane & 31) >> 1) ^ hi) << 4) + 8 * (lane & 1);
  float m = -INFINITY;
  v2u vsr, vzr;

  int ahead = 1;
  int late = -SLACK0;
#define THROTTLE(step)                                                                                                   \
  asm volatile("s_cmp_lt_i32 %0, %1\n\ts_cbranch_scc1 1f\n\ts_waitcnt vmcnt(%3)\n\t"                                     \
               "s_cmp_lt_i32 %0, 0\n\ts_cbranch_scc1 1f\n\ts_waitcnt vmcnt(%2)\n1:"                                      \
               ::"s"(ahead), "s"(late), "i"(waitn(CAP1, step)), "i"(waitn(CAP2, step)) : "scc")
#define SCHED(step)                                                                                                      \
  do {                                                                                                                   \
    if ((step) == 0) { due += TAU; late -= SLACK1; }                                                                     \
    if ((step) % 4 == 0) ahead = (int)(due + (step) * TAU / 16 - NOW());                                                 \
  } while (0)

  f32x4 d0, d1;
  v4u kf[4];
  auto frag_read = [&](int half) __attribute__((always_inline)) {
    __builtin_amdgcn_fence(__ATOMIC_ACQ_REL, "wavefront");
#pragma unroll
    for (int kk = 0; kk < 4; ++kk) kf[kk] = *(LAS v4u*)(kbuf + c * 256 + (((4 * kk + g) ^ c) << 4) + 4096 * half);
    __builtin_amdgcn_fence(__ATOMIC_ACQ_REL, "wavefront");
  };
  auto qk_mfma = [&]() __attribute__((always_inline)) {
    f32x4 d = {0.f, 0.f, 0.f, 0.f};
#pragma unroll
    for (int kk = 0; kk < 4; ++kk) d = mfma16(kf[kk], qf[kk], d);
    return d;
  };
  auto put_logits = [&](int it) __attribute__((always_inline)) {
    const unsigned r01 = pk_bf16(u ? d1[0] : d0[0], u ? d1[1] : d0[1]);
    const unsigned r23 = pk_bf16(u ? d1[2] : d0[2], u ? d1[3] : d0[3]);
    v2u sv;
    sv.x = pk_bf16(bf_lo(r01) * SCALE, bf_hi(r01) * SCALE);
    sv.y = pk_bf16(bf_lo(r23) * SCALE, bf_hi(r23) * SCALE);
    sbuf[it * 64] = sv;
    m = fmaxf(fmaxf(m, fmaxf(bf_lo(sv.x), bf_hi(sv.x))), fmaxf(bf_lo(sv.y), bf_hi(sv.y)));
    asm volatile("" ::"v"(m));
  };
  auto k_unit = [&](auto FIRST_, auto LAST_, int j, int it) __attribute__((always_inline)) {
    constexpr bool FIRST = decltype(FIRST_)::value, LAST = decltype(LAST_)::value;
    const int gb = ubase(j);
    const int jn = LAST ? j0 : (j + UPI) & (NUNIT - 1);
    const int gbn = ubase(jn);
    const float kz0 = bf_lo(kzr.x), kz1 = bf_hi(kzr.x), kz2 = bf_lo(kzr.y), kz3 = bf_hi(kzr.y);
    const float ks0 = bf_lo(ksr.x), ks1 = bf_hi(ksr.x), ks2 = bf_lo(ksr.y), ks3 = bf_hi(ksr.y);
    SB();
#pragma unroll
    for (int p = 0; p < 16; ++p) {
      const v4u x = a[p];
      asm volatile("" ::"v"(x));
      if (!FIRST) { if (!LAST || p < 13) THROTTLE(p); }
      else if (p + DIST >= 16) asm volatile("s_waitcnt vmcnt(%0)" ::"i"(CAP1 - 1 + (p + DIST - CAP1 < 16 ? 2 : 0)));
      SB();
      if (p + DIST < 16) { if (!FIRST) a[p + DIST] = LDK(gb, p + DIST); }
      else if (!LAST) a[(p + DIST) & 15] = LDK(gbn, (p + DIST) & 15);
      else a[(p + DIST) & 15] = LDV(gbn, (p + DIST) & 15);
      if (p == 4) {
        if (LAST) { vsr = ldnt8(rvs, voffT, soff_v(jn)); vzr = ldnt8(rvz, voffT, soff_v(jn)); }
        else { kzr = ldnt8(rkz, voffS, soff_k(jn)); ksr = ldnt8(rks, voffS, soff_k(jn)); }
      }
      SB();
      if (!FIRST) SCHED(p);
      if (!FIRST && p == 0) frag_read(1);
      if (p == 8) frag_read(0);
      const unsigned d01 = pk_bf16((float)(int)x.x - kz0, (float)(int)x.y - kz1);
      const unsigned d23 = pk_bf16((float)(int)x.z - kz2, (float)(int)x.w - kz3);
      v2u kk2;
      kk2.x = pk_bf16(bf_lo(d01) * ks0, bf_hi(d01) * ks1);
      kk2.y = pk_bf16(bf_lo(d23) * ks2, bf_hi(d23) * ks3);
      *(LAS v2u*)(kbuf + (wr0 ^ (32 * (p & 7))) + 512 * p) = kk2;
      if (!FIRST && p == 1) d1 = qk_mfma();
      if (!FIRST && p == 2) put_logits(it - 1);
      if (p == 9) d0 = qk_mfma();
      SB();
    }
  };
  asm volatile("" ::"v"(codew));
  k_unit(TrueT{}, FalseT{}, j0, 0);
#pragma nounroll
  for (int it = 1; it < NIT - 1; ++it) k_unit(FalseT{}, FalseT{}, (j0 + it * UPI) & (NUNIT - 1), it);
  k_unit(FalseT{}, TrueT{}, (j0 + (NIT - 1) * UPI) & (NUNIT - 1), NIT - 1);
  frag_read(1);
  d1 = qk_mfma();
  put_logits(NIT - 1);

  m = fmaxf(m, dpp<ROR8>(m));
  m = xrow_max(m);
  float lsum = 0.f, csum = 0.f;
  f32x4 acc[2][4];
#pragma unroll
  for (int hf = 0; hf < 2; ++hf)
#pragma unroll
    for (int e = 0; e < 4; ++e) acc[hf][e] = f32x4{0.f, 0.f, 0.f, 0.f};
  float wgt[NWAVE], cterm;
  unsigned sideb;
  const int hsh = 16 * (lane & 1);
  const unsigned tagb = ((tag >> hsh) & 0xffu) | (((tag >> hsh) & 0xff00u) << 8);
  LAS float* obuf = (LAS float*)kbuf + c * OSTR + 16 * g;
  auto v_unit = [&](auto LAST_, int j, int it) __attribute__((always_inline)) {
    constexpr bool LAST = decltype(LAST_)::value;
    const int gb = ubase(j);
    const int jn = (j + UPI) & (NUNIT - 1);
    const int gbn = ubase(jn);
    const v2u sv = sbuf[it * 64];
    SB();
    v4u pf, vf[4];
    float p0, p1, p2, p3;
#pragma unroll
    for (int n = 0; n < 16; ++n) {
      if (!LAST) {
        asm volatile("" ::"v"(a[n]));
        THROTTLE(n);
        SB();
        a[(n + DIST) & 15] = LDV(n + DIST < 16 ? gb : gbn, (n + DIST) & 15);
        SB();
        SCHED(n);
      } else if (n < LASTP) {
        asm volatile("" ::"v"(a[n]));
        THROTTLE(0);
        SB();
#pragma unroll
        for (int k = n + DIST; k < (n + 1 < LASTP ? n + DIST + 1 : 16); ++k) a[k] = LDV(gb, k);
        SB();
      }
      if (n & 1) {
#pragma unroll
        for (int e = 0; e < 4; ++e) vf[e][(n >> 1) & 3] = pk_bf16((float)(int)a[n - 1][e], (float)(int)a[n][e]);
        asm volatile("" ::"v"(vf[0][(n >> 1) & 3]), "v"(vf[1][(n >> 1) & 3]), "v"(vf[2][(n >> 1) & 3]), "v"(vf[3][(n >> 1) & 3]));
      }
      if (n == 0) {
        p0 = __builtin_amdgcn_exp2f((bf_lo(sv.x) - m) * LOG2E); p1 = __builtin_amdgcn_exp2f((bf_hi(sv.x) - m) * LOG2E);
        p2 = __builtin_amdgcn_exp2f((bf_lo(sv.y) - m) * LOG2E); p3 = __builtin_amdgcn_exp2f((bf_hi(sv.y) - m) * LOG2E);
        lsum += (p0 + p1) + (p2 + p3);
        asm volatile("" ::"v"(lsum));
      }
      if (n == 2) {
        const unsigned p01 = pk_bf16(p0 * bf_lo(vsr.x), p1 * bf_hi(vsr.x));
        const unsigned p23 = pk_bf16(p2 * bf_lo(vsr.y), p3 * bf_hi(vsr.y));
        csum = fmaf(bf_lo(p01), bf_lo(vzr.x), csum);
        csum = fmaf(bf_hi(p01), bf_hi(vzr.x), csum);
        csum = fmaf(bf_lo(p23), bf_lo(vzr.y), csum);
        csum = fmaf(bf_hi(p23), bf_hi(vzr.y), csum);
        pf.x = u ? 0u : p01;
        pf.y = u ? 0u : p23;
        pf.z = u ? p01 : 0u;
        pf.w = u ? p23 : 0u;
        asm volatile("" ::"v"(pf), "v"(csum));
      }
      if (!LAST && n == 4) {
        vsr = ldnt8(rvs, voffT, soff_v(jn));
        vzr = ldnt8(rvz, voffT, soff_v(jn));
      }
      if (LAST && n == LASTP - 1) {
        const float lw = xrow_sum(lsum + dpp<ROR8>(lsum)), cw = xrow_sum(csum + dpp<ROR8>(csum));
        if (lane < 8) mbuf[w * 8 + h] = f32x4{m, lw, cw, 0.f};
        __syncthreads();
        f32x4 ml[NWAVE];
#pragma unroll
        for (int i = 0; i < NWAVE; ++i) ml[i] = mbuf[i * 8 + 2 * w + hi];
        const float M = fmaxf(fmaxf(ml[0][0], ml[1][0]), fmaxf(ml[2][0], ml[3][0]));
        float L = 0.f, C = 0.f;
#pragma unroll
        for (int i = 0; i < NWAVE; ++i) {
          wgt[i] = __builtin_amdgcn_exp2f((ml[i][0] - M) * LOG2E);
          L = fmaf(wgt[i], ml[i][1], L);
          C = fmaf(wgt[i], ml[i][2], C);
        }
        const float inv = __builtin_amdgcn_rcpf(L);
#pragma unroll
        for (int i = 0; i < NWAVE; ++i) wgt[i] *= inv;
        cterm = -C * inv;
        const unsigned lh = __float_as_uint(fmaf(M, LOG2E, __builtin_amdgcn_logf(L))) >> hsh;
        sideb = tagb | ((lh & 0xffu) << 8) | ((lh & 0xff00u) << 16);
        asm volatile("" ::"v"(wgt[0]), "v"(wgt[1]), "v"(wgt[2]), "v"(wgt[3]), "v"(cterm), "v"(sideb));
      }
      if (LAST && n == 10) {
#pragma unroll
        for (int e = 0; e < 4; ++e) *(LAS f32x4*)(obuf + 4 * e) = acc[0][e];
      }
      if ((n & 7) == 7) {
#pragma unroll
        for (int e = 0; e < 4; ++e) acc[n >> 3][e] = mfma16(vf[e], pf, acc[n >> 3][e]);
      }
      SB();
    }
  };
#pragma nounroll
  for (int it = 0; it < NIT - 1; ++it) v_unit(FalseT{}, (j0 + it * UPI) & (NUNIT - 1), it);
  v_unit(TrueT{}, (j0 + (NIT - 1) * UPI) & (NUNIT - 1), NIT - 1);
#pragma unroll
  for (int e = 0; e < 4; ++e) *(LAS f32x4*)(obuf + 64 + 4 * e) = acc[1][e];
  __syncthreads();

  {
    f32x4 ob[NWAVE][2];
#pragma unroll
    for (int i = 0; i < NWAVE; ++i) {
      const LAS float* op = (const LAS float*)((LAS unsigned char*)smem + i * WBYTES) + (2 * w + hi) * OSTR + 4 * (lane & 31);
      ob[i][0] = *(const LAS f32x4*)op;
      ob[i][1] = *(const LAS f32x4*)(op + 8 * OSTR);
    }
    f32x4 o = {cterm, cterm, cterm, cterm};
#pragma unroll
    for (int i = 0; i < NWAVE; ++i) o += wgt[i] * (ob[i][0] + ob[i][1]);
    const v4u pk = {(__float_as_uint(o[0]) & 0xffffff00u) | (sideb & 0xffu), (__float_as_uint(o[1]) & 0xffffff00u) | ((sideb >> 8) & 0xffu),
                    (__float_as_uint(o[2]) & 0xffffff00u) | ((sideb >> 16) & 0xffu), (__float_as_uint(o[3]) & 0xffffff00u) | (sideb >> 24)};
    const int cp = 8 * w + (lane >> 3);
    __builtin_amdgcn_raw_buffer_store_b128(pk, rp, (cp * NSPLIT + ((cb + cp) & (NSPLIT - 1))) * 128 + (lane & 7) * 16, 0, AUX_SC1);
  }
  asm volatile("s_waitcnt vmcnt(0)" ::: "memory");
  __builtin_amdgcn_s_sleep(4);
  while ((int)(due + TAU + GATE - NOW()) > 0) __builtin_amdgcn_s_sleep(1);

  {
    int po = ((cb >> 1) * NSPLIT + 16 * w + (lane >> 2)) * 128 + (cb & 1) * 64 + (lane & 3) * 16;
    v4u xo = {0u, 0u, 0u, 0u};
    unsigned spins = 0;
    bool bad = false;
    while (true) {
      asm volatile("" ::: "memory");
      const v4u x = __builtin_amdgcn_raw_buffer_load_b128(rp, po, 0, AUX_SC1);
      unsigned long long okm = __ballot(((x.x & 0xffu) | ((x.z & 0xffu) << 16)) == tagb);
      okm &= okm >> 1;
      okm &= okm >> 2;
      okm = (okm & 0x1111111111111111ull) * 15ull;
      const bool k = (okm >> lane) & 1ull;
      xo = k ? x : xo;
      po = k ? OOB : po;
      if (__all(po == OOB)) break;
      if (++spins > SPIN_CAP) { bad = true; break; }
      __builtin_amdgcn_s_sleep(2);
    }
    const unsigned lhalf = ((xo.y & 0xffu) | ((xo.w & 0xffu) << 8)) << hsh;
    float lse = __uint_as_float(lhalf | (unsigned)__builtin_amdgcn_mov_dpp((int)lhalf, XOR1, 0xf, 0xf, true));
    if (bad) lse = __builtin_nanf("");
    float M = fmaxf(lse, dpp<ROR4>(lse));
    M = fmaxf(M, dpp<ROR8>(M));
    M = xrow_max(M);
    const float e = __builtin_amdgcn_exp2f(lse - M);
    float av[4];
#pragma unroll
    for (int i = 0; i < 4; ++i) av[i] = e * __uint_as_float(xo[i] & 0xffffff00u);
    auto s02 = __builtin_amdgcn_permlane32_swap(__float_as_uint(av[0]), __float_as_uint(av[2]), false, false);
    auto s13 = __builtin_amdgcn_permlane32_swap(__float_as_uint(av[1]), __float_as_uint(av[3]), false, false);
    const float b0 = __uint_as_float(s02[0]) + __uint_as_float(s02[1]);
    const float b1 = __uint_as_float(s13[0]) + __uint_as_float(s13[1]);
    auto sw = __builtin_amdgcn_permlane16_swap(__float_as_uint(b0), __float_as_uint(b1), false, false);
    float t = __uint_as_float(sw[0]) + __uint_as_float(sw[1]);
    t += dpp<ROR4>(t);
    t += dpp<ROR8>(t);
    float L = e + dpp<ROR4>(e);
    L = xrow_sum(L + dpp<ROR8>(L));
    LAS f32x4* fin = (LAS f32x4*)((LAS unsigned char*)smem + NWAVE * WBYTES + NWAVE * 8 * 16);
    if (c < 4) fin[w * 16 + 4 * g + c] = f32x4{t, M, L, 0.f};
    __syncthreads();
    if (tid < 16) {
      f32x4 r[NWAVE];
#pragma unroll
      for (int i = 0; i < NWAVE; ++i) r[i] = fin[i * 16 + tid];
      const float Mx = fmaxf(fmaxf(r[0][1], r[1][1]), fmaxf(r[2][1], r[3][1]));
      float Lx = 0.f, ox = 0.f;
#pragma unroll
      for (int i = 0; i < NWAVE; ++i) {
        const float ee = __builtin_amdgcn_exp2f(r[i][1] - Mx);
        Lx = fmaf(ee, r[i][2], Lx);
        ox = fmaf(ee, r[i][0], ox);
      }
      __builtin_amdgcn_raw_buffer_store_b16((unsigned short)pk_bf16(ox * __builtin_amdgcn_rcpf(Lx), 0.f), RSRC(out, HKV * 8 * DH * 2),
                                            ((hk * 8 + (cb >> 3)) * DH + 16 * (cb & 7) + tid) * 2, 0, AUX_SC1);
      if (cb == 0 && tid == 0) __hip_atomic_store((gu32*)ws + hk * 64, tag, __ATOMIC_RELAXED, __HIP_MEMORY_SCOPE_AGENT);
    }
  }
}
}

extern "C" void kernel_launch(void* const* d_in, const int* in_sizes, int n_in, void* d_out, int out_size, void* d_ws,
                              size_t ws_size, hipStream_t stream) {
  kv_decode<<<HKV * NSPLIT, NTHR, 0, stream>>>((const unsigned short*)d_in[0], (const int*)d_in[1], (const int*)d_in[2],
                                               (const unsigned short*)d_in[3], (const unsigned short*)d_in[4],
                                               (const unsigned short*)d_in[5], (const unsigned short*)d_in[6],
                                               (unsigned char*)d_ws, (unsigned short*)d_out);
}
```

```cpp
#include <hip/hip_runtime.h>
#include <hip/hip_bf16.h>
#include <stdint.h>

namespace {
constexpr int HKV = 4, SKV = 65536, DH = 128, GS = 128, NGRP = SKV / GS;
constexpr int NWAVE = 4, NTHR = NWAVE * 64;
constexpr int NSPLIT = 64;
constexpr int NUNIT = NGRP * 4;
constexpr int UPI = NSPLIT * NWAVE;
constexpr int NIT = NUNIT / UPI;
constexpr int OSTR = 132;
constexpr int WBYTES = 16 * OSTR * 4;
constexpr size_t WS_PAY = 4096;
constexpr unsigned SPIN_CAP = 1u << 20;
constexpr int OOB = 0x40000000;
constexpr int DIST = 8;
constexpr int CAP1 = 5, CAP2 = 6;
constexpr int T_UNIT1 = 358, TAU = 217;
constexpr int SLACK0 = 42, SLACK1 = 4;
constexpr int GATE = 208;
constexpr int LASTP = 5;
constexpr int CODE_BYTES = 14600;

typedef unsigned v4u __attribute__((ext_vector_type(4)));
typedef unsigned v2u __attribute__((ext_vector_type(2)));
typedef float f32x4 __attribute__((ext_vector_type(4)));
typedef float f32x2 __attribute__((ext_vector_type(2)));
typedef __bf16 bf16x8 __attribute__((ext_vector_type(8)));
typedef __bf16 bf16x2 __attribute__((ext_vector_type(2)));
#define LAS __attribute__((address_space(3)))
typedef __attribute__((address_space(1))) unsigned gu32;
#define RSRC(p, n) __builtin_amdgcn_make_buffer_rsrc((void*)(p), 0, (n), 0x00020000)
constexpr int AUX_NT = 2, AUX_SC1 = 16;
#define SB() __builtin_amdgcn_sched_barrier(0)
#define NOW() ((unsigned)__builtin_amdgcn_s_memrealtime())

__device__ __forceinline__ unsigned pk_bf16(float lo, float hi) {
  f32x2 v = {lo, hi};
  return __builtin_bit_cast(unsigned, __builtin_convertvector(v, bf16x2));
}
__device__ __forceinline__ float bf_lo(unsigned p) { return __uint_as_float(p << 16); }
__device__ __forceinline__ float bf_hi(unsigned p) { return __uint_as_float(p & 0xffff0000u); }
__device__ __forceinline__ f32x4 mfma16(v4u a, v4u b, f32x4 c) {
  return __builtin_amdgcn_mfma_f32_16x16x32_bf16(__builtin_bit_cast(bf16x8, a), __builtin_bit_cast(bf16x8, b), c, 0, 0, 0);
}
__device__ __forceinline__ v4u ldnt(__amdgpu_buffer_rsrc_t rs, int voff, int soff) {
  return __builtin_amdgcn_raw_buffer_load_b128(rs, voff, soff, AUX_NT);
}
__device__ __forceinline__ v2u ldnt8(__amdgpu_buffer_rsrc_t rs, int voff, int soff) {
  return __builtin_amdgcn_raw_buffer_load_b64(rs, voff, soff, AUX_NT);
}
template <int CTRL> __device__ __forceinline__ float dpp(float x) {
  return __int_as_float(__builtin_amdgcn_mov_dpp(__float_as_int(x), CTRL, 0xf, 0xf, true));
}
constexpr int ROR4 = 0x124, ROR8 = 0x128, XOR1 = 0xB1;
__device__ __forceinline__ float xrow_max(float x) {
  auto s = __builtin_amdgcn_permlane16_swap(__float_as_uint(x), __float_as_uint(x), false, false);
  x = fmaxf(__uint_as_float(s[0]), __uint_as_float(s[1]));
  auto t = __builtin_amdgcn_permlane32_swap(__float_as_uint(x), __float_as_uint(x), false, false);
  return fmaxf(__uint_as_float(t[0]), __uint_as_float(t[1]));
}
__device__ __forceinline__ float xrow_sum(float x) {
  auto s = __builtin_amdgcn_permlane16_swap(__float_as_uint(x), __float_as_uint(x), false, false);
  x = __uint_as_float(s[0]) + __uint_as_float(s[1]);
  auto t = __builtin_amdgcn_permlane32_swap(__float_as_uint(x), __float_as_uint(x), false, false);
  return __uint_as_float(t[0]) + __uint_as_float(t[1]);
}
__device__ __forceinline__ int ubase(int j) { return (j >> 1) * 32768 + (j & 1) * 2048; }
__device__ __forceinline__ int soff_k(int j) { return (j >> 2) * 256; }
__device__ __forceinline__ int soff_v(int j) { return (j >> 1) * 128 + (j & 1) * 8; }
constexpr int waitn(int c, int step) { return c - 1 + ((step > 4 && step <= 4 + c) ? 2 : 0); }

struct TrueT { static constexpr bool value = true; };
struct FalseT { static constexpr bool value = false; };
constexpr float LOG2E = 1.4426950408889634f;
constexpr float SCALE = 0.08838834764831845f;

__global__ void __launch_bounds__(NTHR, 2)
kv_decode(const unsigned short* __restrict__ q, const int* __restrict__ qk, const int* __restrict__ qv,
          const unsigned short* __restrict__ ks, const unsigned short* __restrict__ kz,
          const unsigned short* __restrict__ vs, const unsigned short* __restrict__ vz,
          unsigned char* ws, unsigned short* __restrict__ out) {
  __shared__ __attribute__((aligned(16))) unsigned char smem[NWAVE * WBYTES + NWAVE * 8 * 16 + NWAVE * NIT * 64 * 8];
  const int tid = threadIdx.x, lane = tid & 63;
  unsigned long long pc0;
  asm volatile("s_getpc_b64 %0" : "=s"(pc0));
  const unsigned codew = __builtin_amdgcn_raw_buffer_load_b32(
      RSRC(pc0 & ~255ull, CODE_BYTES), ((tid & 31) + 32 * ((blockIdx.x >> 3) & 15)) * 128 | (tid & 224) << 22, 0, 0);
  SB();
  const int w = __builtin_amdgcn_readfirstlane(tid >> 6);
  const int hk = blockIdx.x & 3, cb = (blockIdx.x >> 3) + 32 * ((blockIdx.x >> 2) & 1);
  const int rot = ((blockIdx.x >> 3) + 2 * w) & 7;
  const int j0 = (cb * NWAVE + w + 2 * hk * UPI) & (NUNIT - 1);
  const int voffK = lane * 16;
  asm volatile("" ::"s"(ubase(j0) + (rot << 12)), "s"(ubase(j0) + ((1 ^ rot) << 12)), "v"(voffK));
  SB();
  asm volatile("" ::"s"(qk), "s"(kz), "s"(vz), "s"(out));
  constexpr int HEADB = SKV * DH * 4;
  const __amdgpu_buffer_rsrc_t rk = RSRC((const char*)qk + (size_t)hk * HEADB, HEADB);
#define LDK(gb, p) ldnt(rk, voffK + ((p) & 1) * 1024, (gb) + ((((p) >> 1) ^ rot) << 12))
#define LDV(gb, n) ldnt(rv, (((n) & 4) ? voffV1 : voffV0) + ((n) & 3) * 512 + ((n) >> 3) * 256, (gb))
  v4u a[16];
#pragma unroll
  for (int i = 0; i < 4; ++i) a[i] = LDK(ubase(j0), i);
  SB();
  const unsigned long long tbeg = __builtin_amdgcn_s_memrealtime();
  const __amdgpu_buffer_rsrc_t rks = RSRC(ks + (size_t)hk * NGRP * DH, NGRP * DH * 2);
  const __amdgpu_buffer_rsrc_t rkz = RSRC(kz + (size_t)hk * NGRP * DH, NGRP * DH * 2);
  const int voffS = 8 * (lane & 31);
  v2u kzr = ldnt8(rkz, voffS, soff_k(j0));
  v2u ksr = ldnt8(rks, voffS, soff_k(j0));
#pragma unroll
  for (int i = 4; i < 8; ++i) a[i] = LDK(ubase(j0), i);
  SB();
  const int g = lane >> 4, c = lane & 15, u = (lane >> 3) & 1, h = lane & 7, hi = lane >> 5;
  v4u qf[4];
#pragma unroll
  for (int kk = 0; kk < 4; ++kk) qf[kk] = __builtin_nontemporal_load((const v4u*)(q + (size_t)(hk * 8 + h) * DH + 32 * kk + 8 * g));
  SB();
#pragma unroll
  for (int i = 8; i < 16; ++i) a[i] = LDK(ubase(j0), i);
  SB();
  asm volatile("" ::"s"(tbeg));
  unsigned due = (unsigned)tbeg + T_UNIT1 - TAU;
  unsigned tag = *(const volatile __attribute__((address_space(4))) unsigned*)(ws + hk * 256) + 0x01010101u;
  tag = tag ? tag : 0x01010101u;
  SB();

  const __amdgpu_buffer_rsrc_t rp = RSRC(ws + WS_PAY + (size_t)hk * NSPLIT * NSPLIT * 64, NSPLIT * NSPLIT * 64);
  const __amdgpu_buffer_rsrc_t rv = RSRC((const char*)qv + (size_t)hk * HEADB, HEADB);
  const __amdgpu_buffer_rsrc_t rvs = RSRC(vs + (size_t)hk * SKV, SKV * 2);
  const __amdgpu_buffer_rsrc_t rvz = RSRC(vz + (size_t)hk * SKV, SKV * 2);
  const int voffV0 = ((g ^ rot) << 12) + c * 16, voffV1 = (((4 + g) ^ rot) << 12) + c * 16;
  const int voffT = 16 * ((4 * u + g) ^ rot);
  LAS unsigned char* kbuf = (LAS unsigned char*)smem + w * WBYTES;
  LAS f32x4* mbuf = (LAS f32x4*)((LAS unsigned char*)smem + NWAVE * WBYTES);
  LAS v2u* sbuf = (LAS v2u*)((LAS unsigned char*)smem + NWAVE * WBYTES + NWAVE * 8 * 16) + w * (NIT * 64) + lane;
  const int wr0 = hi * 256 + ((((lane & 31) >> 1) ^ hi) << 4) + 8 * (lane & 1);
  float m = -INFINITY;
  v2u vsr, vzr;

  int ahead = 1;
  int late = -SLACK0;
#define THROTTLE(step)                                                                                                   \
  asm volatile("s_cmp_lt_i32 %0, %1\n\ts_cbranch_scc1 1f\n\ts_waitcnt vmcnt(%3)\n\t"                                     \
               "s_cmp_lt_i32 %0, 0\n\ts_cbranch_scc1 1f\n\ts_waitcnt vmcnt(%2)\n1:"                                      \
               ::"s"(ahead), "s"(late), "i"(waitn(CAP1, step)), "i"(waitn(CAP2, step)) : "scc")
#define SCHED(step)                                                                                                      \
  do {                                                                                                                   \
    if ((step) == 0) { due += TAU; late -= SLACK1; }                                                                     \
    if ((step) % 4 == 0) ahead = (int)(due + (step) * TAU / 16 - NOW());                                                 \
  } while (0)

  f32x4 d0, d1;
  v4u kf[4];
  auto frag_read = [&](int half) __attribute__((always_inline)) {
    __builtin_amdgcn_fence(__ATOMIC_ACQ_REL, "wavefront");
#pragma unroll
    for (int kk = 0; kk < 4; ++kk) kf[kk] = *(LAS v4u*)(kbuf + c * 256 + (((4 * kk + g) ^ c) << 4) + 4096 * half);
    __builtin_amdgcn_fence(__ATOMIC_ACQ_REL, "wavefront");
  };
  auto qk_mfma = [&]() __attribute__((always_inline)) {
    f32x4 d = {0.f, 0.f, 0.f, 0.f};
#pragma unroll
    for (int kk = 0; kk < 4; ++kk) d = mfma16(kf[kk], qf[kk], d);
    return d;
  };
  auto put_logits = [&](int it) __attribute__((always_inline)) {
    const unsigned r01 = pk_bf16(u ? d1[0] : d0[0], u ? d1[1] : d0[1]);
    const unsigned r23 = pk_bf16(u ? d1[2] : d0[2], u ? d1[3] : d0[3]);
    v2u sv;
    sv.x = pk_bf16(bf_lo(r01) * SCALE, bf_hi(r01) * SCALE);
    sv.y = pk_bf16(bf_lo(r23) * SCALE, bf_hi(r23) * SCALE);
    sbuf[it * 64] = sv;
    m = fmaxf(fmaxf(m, fmaxf(bf_lo(sv.x), bf_hi(sv.x))), fmaxf(bf_lo(sv.y), bf_hi(sv.y)));
    asm volatile("" ::"v"(m));
  };
  auto k_unit = [&](auto FIRST_, auto LAST_, int j, int it) __attribute__((always_inline)) {
    constexpr bool FIRST = decltype(FIRST_)::value, LAST = decltype(LAST_)::value;
    const int gb = ubase(j);
    const int jn = LAST ? j0 : (j + UPI) & (NUNIT - 1);
    const int gbn = ubase(jn);
    const float kz0 = bf_lo(kzr.x), kz1 = bf_hi(kzr.x), kz2 = bf_lo(kzr.y), kz3 = bf_hi(kzr.y);
    const float ks0 = bf_lo(ksr.x), ks1 = bf_hi(ksr.x), ks2 = bf_lo(ksr.y), ks3 = bf_hi(ksr.y);
    SB();
#pragma unroll
    for (int p = 0; p < 16; ++p) {
      const v4u x = a[p];
      asm volatile("" ::"v"(x));
      if (!FIRST) { if (!LAST || p < 13) THROTTLE(p); }
      else if (p + DIST >= 16) asm volatile("s_waitcnt vmcnt(%0)" ::"i"(CAP1 - 1 + (p + DIST - CAP1 < 16 ? 2 : 0)));
      SB();
      if (p + DIST < 16) { if (!FIRST) a[p + DIST] = LDK(gb, p + DIST); }
      else if (!LAST) a[(p + DIST) & 15] = LDK(gbn, (p + DIST) & 15);
      else a[(p + DIST) & 15] = LDV(gbn, (p + DIST) & 15);
      if (p == 4) {
        if (LAST) { vsr = ldnt8(rvs, voffT, soff_v(jn)); vzr = ldnt8(rvz, voffT, soff_v(jn)); }
        else { kzr = ldnt8(rkz, voffS, soff_k(jn)); ksr = ldnt8(rks, voffS, soff_k(jn)); }
      }
      SB();
      if (!FIRST) SCHED(p);
      if (!FIRST && p == 0) frag_read(1);
      if (p == 8) frag_read(0);
      const unsigned d01 = pk_bf16((float)(int)x.x - kz0, (float)(int)x.y - kz1);
      const unsigned d23 = pk_bf16((float)(int)x.z - kz2, (float)(int)x.w - kz3);
      v2u kk2;
      kk2.x = pk_bf16(bf_lo(d01) * ks0, bf_hi(d01) * ks1);
      kk2.y = pk_bf16(bf_lo(d23) * ks2, bf_hi(d23) * ks3);
      *(LAS v2u*)(kbuf + (wr0 ^ (32 * (p & 7))) + 512 * p) = kk2;
      if (!FIRST && p == 1) d1 = qk_mfma();
      if (!FIRST && p == 2) put_logits(it - 1);
      if (p == 9) d0 = qk_mfma();
      SB();
    }
  };
  asm volatile("" ::"v"(codew));
  k_unit(TrueT{}, FalseT{}, j0, 0);
#pragma nounroll
  for (int it = 1; it < NIT - 1; ++it) k_unit(FalseT{}, FalseT{}, (j0 + it * UPI) & (NUNIT - 1), it);
  k_unit(FalseT{}, TrueT{}, (j0 + (NIT - 1) * UPI) & (NUNIT - 1), NIT - 1);
  frag_read(1);
  d1 = qk_mfma();
  put_logits(NIT - 1);

  m = fmaxf(m, dpp<ROR8>(m));
  m = xrow_max(m);
  float lsum = 0.f, csum = 0.f;
  f32x4 acc[2][4];
#pragma unroll
  for (int hf = 0; hf < 2; ++hf)
#pragma unroll
    for (int e = 0; e < 4; ++e) acc[hf][e] = f32x4{0.f, 0.f, 0.f, 0.f};
  float wgt[NWAVE], cterm;
  unsigned sideb;
  const int hsh = 16 * (lane & 1);
  const unsigned tagb = ((tag >> hsh) & 0xffu) | (((tag >> hsh) & 0xff00u) << 8);
  LAS float* obuf = (LAS float*)kbuf + c * OSTR + 16 * g;
  auto v_unit = [&](auto LAST_, int j, int it) __attribute__((always_inline)) {
    constexpr bool LAST = decltype(LAST_)::value;
    const int gb = ubase(j);
    const int jn = (j + UPI) & (NUNIT - 1);
    const int gbn = ubase(jn);
    const v2u sv = sbuf[it * 64];
    SB();
    v4u pf, vf[4];
    float p0, p1, p2, p3;
#pragma unroll
    for (int n = 0; n < 16; ++n) {
      if (!LAST) {
        asm volatile("" ::"v"(a[n]));
        THROTTLE(n);
        SB();
        a[(n + DIST) & 15] = LDV(n + DIST < 16 ? gb : gbn, (n + DIST) & 15);
        SB();
        SCHED(n);
      } else if (n < LASTP) {
        asm volatile("" ::"v"(a[n]));
        THROTTLE(0);
        SB();
#pragma unroll
        for (int k = n + DIST; k < (n + 1 < LASTP ? n + DIST + 1 : 16); ++k) a[k] = LDV(gb, k);
        SB();
      }
      if (n & 1) {
#pragma unroll
        for (int e = 0; e < 4; ++e) vf[e][(n >> 1) & 3] = pk_bf16((float)(int)a[n - 1][e], (float)(int)a[n][e]);
        asm volatile("" ::"v"(vf[0][(n >> 1) & 3]), "v"(vf[1][(n >> 1) & 3]), "v"(vf[2][(n >> 1) & 3]), "v"(vf[3][(n >> 1) & 3]));
      }
      if (n == 0) {
        p0 = __builtin_amdgcn_exp2f((bf_lo(sv.x) - m) * LOG2E); p1 = __builtin_amdgcn_exp2f((bf_hi(sv.x) - m) * LOG2E);
        p2 = __builtin_amdgcn_exp2f((bf_lo(sv.y) - m) * LOG2E); p3 = __builtin_amdgcn_exp2f((bf_hi(sv.y) - m) * LOG2E);
        lsum += (p0 + p1) + (p2 + p3);
        asm volatile("" ::"v"(lsum));
      }
      if (n == 2) {
        const unsigned p01 = pk_bf16(p0 * bf_lo(vsr.x), p1 * bf_hi(vsr.x));
        const unsigned p23 = pk_bf16(p2 * bf_lo(vsr.y), p3 * bf_hi(vsr.y));
        csum = fmaf(bf_lo(p01), bf_lo(vzr.x), csum);
        csum = fmaf(bf_hi(p01), bf_hi(vzr.x), csum);
        csum = fmaf(bf_lo(p23), bf_lo(vzr.y), csum);
        csum = fmaf(bf_hi(p23), bf_hi(vzr.y), csum);
        pf.x = u ? 0u : p01;
        pf.y = u ? 0u : p23;
        pf.z = u ? p01 : 0u;
        pf.w = u ? p23 : 0u;
        asm volatile("" ::"v"(pf), "v"(csum));
      }
      if (!LAST && n == 4) {
        vsr = ldnt8(rvs, voffT, soff_v(jn));
        vzr = ldnt8(rvz, voffT, soff_v(jn));
      }
      if (LAST && n == LASTP - 1) {
        const float lw = xrow_sum(lsum + dpp<ROR8>(lsum)), cw = xrow_sum(csum + dpp<ROR8>(csum));
        if (lane < 8) mbuf[w * 8 + h] = f32x4{m, lw, cw, 0.f};
        __syncthreads();
        f32x4 ml[NWAVE];
#pragma unroll
        for (int i = 0; i < NWAVE; ++i) ml[i] = mbuf[i * 8 + 2 * w + hi];
        const float M = fmaxf(fmaxf(ml[0][0], ml[1][0]), fmaxf(ml[2][0], ml[3][0]));
        float L = 0.f, C = 0.f;
#pragma unroll
        for (int i = 0; i < NWAVE; ++i) {
          wgt[i] = __builtin_amdgcn_exp2f((ml[i][0] - M) * LOG2E);
          L = fmaf(wgt[i], ml[i][1], L);
          C = fmaf(wgt[i], ml[i][2], C);
        }
        const float inv = __builtin_amdgcn_rcpf(L);
#pragma unroll
        for (int i = 0; i < NWAVE; ++i) wgt[i] *= inv;
        cterm = -C * inv;
        const unsigned lh = __float_as_uint(fmaf(M, LOG2E, __builtin_amdgcn_logf(L))) >> hsh;
        sideb = tagb | ((lh & 0xffu) << 8) | ((lh & 0xff00u) << 16);
        asm volatile("" ::"v"(wgt[0]), "v"(wgt[1]), "v"(wgt[2]), "v"(wgt[3]), "v"(cterm), "v"(sideb));
      }
      if (LAST && n == 10) {
#pragma unroll
        for (int e = 0; e < 4; ++e) *(LAS f32x4*)(obuf + 4 * e) = acc[0][e];
      }
      if ((n & 7) == 7) {
#pragma unroll
        for (int e = 0; e < 4; ++e) acc[n >> 3][e] = mfma16(vf[e], pf, acc[n >> 3][e]);
      }
      SB();
    }
  };
#pragma nounroll
  for (int it = 0; it < NIT - 1; ++it) v_unit(FalseT{}, (j0 + it * UPI) & (NUNIT - 1), it);
  v_unit(TrueT{}, (j0 + (NIT - 1) * UPI) & (NUNIT - 1), NIT - 1);
#pragma unroll
  for (int e = 0; e < 4; ++e) *(LAS f32x4*)(obuf + 64 + 4 * e) = acc[1][e];
  __syncthreads();

  {
    f32x4 ob[NWAVE][2];
#pragma unroll
    for (int i = 0; i < NWAVE; ++i) {
      const LAS float* op = (const LAS float*)((LAS unsigned char*)smem + i * WBYTES) + (2 * w + hi) * OSTR + 4 * (lane & 31);
      ob[i][0] = *(const LAS f32x4*)op;
      ob[i][1] = *(const LAS f32x4*)(op + 8 * OSTR);
    }
    f32x4 o = {cterm, cterm, cterm, cterm};
#pragma unroll
    for (int i = 0; i < NWAVE; ++i) o += wgt[i] * (ob[i][0] + ob[i][1]);
    const v4u pk = {(__float_as_uint(o[0]) & 0xffffff00u) | (sideb & 0xffu), (__float_as_uint(o[1]) & 0xffffff00u) | ((sideb >> 8) & 0xffu),
                    (__float_as_uint(o[2]) & 0xffffff00u) | ((sideb >> 16) & 0xffu), (__float_as_uint(o[3]) & 0xffffff00u) | (sideb >> 24)};
    const int cp = 8 * w + (lane >> 3);
    __builtin_amdgcn_raw_buffer_store_b128(pk, rp, (cp * NSPLIT + ((cb + cp) & (NSPLIT - 1))) * 128 + (lane & 7) * 16, 0, AUX_SC1);
  }
  asm volatile("s_waitcnt vmcnt(0)" ::: "memory");
  __builtin_amdgcn_s_sleep(2);
  while ((int)(due + TAU + GATE - NOW()) > 0) __builtin_amdgcn_s_sleep(1);

  {
    int po = ((cb >> 1) * NSPLIT + 16 * w + (lane >> 2)) * 128 + (cb & 1) * 64 + (lane & 3) * 16;
    v4u xo = {0u, 0u, 0u, 0u};
    unsigned spins = 0;
    bool bad = false;
    while (true) {
      asm volatile("" ::: "memory");
      const v4u x = __builtin_amdgcn_raw_buffer_load_b128(rp, po, 0, AUX_SC1);
      unsigned long long okm = __ballot(((x.x & 0xffu) | ((x.z & 0xffu) << 16)) == tagb);
      okm &= okm >> 1;
      okm &= okm >> 2;
      okm = (okm & 0x1111111111111111ull) * 15ull;
      const bool k = (okm >> lane) & 1ull;
      xo = k ? x : xo;
      po = k ? OOB : po;
      if (__all(po == OOB)) break;
      if (++spins > SPIN_CAP) { bad = true; break; }
      __builtin_amdgcn_s_sleep(2);
    }
    const unsigned lhalf = ((xo.y & 0xffu) | ((xo.w & 0xffu) << 8)) << hsh;
    float lse = __uint_as_float(lhalf | (unsigned)__builtin_amdgcn_mov_dpp((int)lhalf, XOR1, 0xf, 0xf, true));
    if (bad) lse = __builtin_nanf("");
    float M = fmaxf(lse, dpp<ROR4>(lse));
    M = fmaxf(M, dpp<ROR8>(M));
    M = xrow_max(M);
    const float e = __builtin_amdgcn_exp2f(lse - M);
    float av[4];
#pragma unroll
    for (int i = 0; i < 4; ++i) av[i] = e * __uint_as_float(xo[i] & 0xffffff00u);
    auto s02 = __builtin_amdgcn_permlane32_swap(__float_as_uint(av[0]), __float_as_uint(av[2]), false, false);
    auto s13 = __builtin_amdgcn_permlane32_swap(__float_as_uint(av[1]), __float_as_uint(av[3]), false, false);
    const float b0 = __uint_as_float(s02[0]) + __uint_as_float(s02[1]);
    const float b1 = __uint_as_float(s13[0]) + __uint_as_float(s13[1]);
    auto sw = __builtin_amdgcn_permlane16_swap(__float_as_uint(b0), __float_as_uint(b1), false, false);
    float t = __uint_as_float(sw[0]) + __uint_as_float(sw[1]);
    t += dpp<ROR4>(t);
    t += dpp<ROR8>(t);
    float L = e + dpp<ROR4>(e);
    L = xrow_sum(L + dpp<ROR8>(L));
    LAS f32x4* fin = (LAS f32x4*)((LAS unsigned char*)smem + NWAVE * WBYTES + NWAVE * 8 * 16);
    if (c < 4) fin[w * 16 + 4 * g + c] = f32x4{t, M, L, 0.f};
    __syncthreads();
    if (tid < 16) {
      f32x4 r[NWAVE];
#pragma unroll
      for (int i = 0; i < NWAVE; ++i) r[i] = fin[i * 16 + tid];
      const float Mx = fmaxf(fmaxf(r[0][1], r[1][1]), fmaxf(r[2][1], r[3][1]));
      float Lx = 0.f, ox = 0.f;
#pragma unroll
      for (int i = 0; i < NWAVE; ++i) {
        const float ee = __builtin_amdgcn_exp2f(r[i][1] - Mx);
        Lx = fmaf(ee, r[i][2], Lx);
        ox = fmaf(ee, r[i][0], ox);
      }
      __builtin_amdgcn_raw_buffer_store_b16((unsigned short)pk_bf16(ox * __builtin_amdgcn_rcpf(Lx), 0.f), RSRC(out, HKV * 8 * DH * 2),
                                            ((hk * 8 + (cb >> 3)) * DH + 16 * (cb & 7) + tid) * 2, 0, AUX_SC1);
      if (cb == 0 && tid == 0) __hip_atomic_store((gu32*)ws + hk * 64, tag, __ATOMIC_RELAXED, __HIP_MEMORY_SCOPE_AGENT);
    }
  }
}
}

extern "C" void kernel_launch(void* const* d_in, const int* in_sizes, int n_in, void* d_out, int out_size, void* d_ws,
                              size_t ws_size, hipStream_t stream) {
  kv_decode<<<HKV * NSPLIT, NTHR, 0, stream>>>((const unsigned short*)d_in[0], (const int*)d_in[1], (const int*)d_in[2],
                                               (const unsigned short*)d_in[3], (const unsigned short*)d_in[4],
                                               (const unsigned short*)d_in[5], (const unsigned short*)d_in[6],
                                               (unsigned char*)d_ws, (unsigned short*)d_out);
}
```
